```python
import torch

def _softmax(x, axis=-1):
    return torch.softmax(x, dim=axis)
def _gelu(x):
    return torch.nn.functional.gelu(x, approximate="tanh")
def _silu(x):
    return torch.nn.functional.silu(x)
def _sigmoid(x):
    return torch.sigmoid(x)
def _layernorm(x, axis=-1, eps=1e-5):
    m = x.mean(dim=axis, keepdim=True)
    v = x.var(dim=axis, keepdim=True, unbiased=False)
    return (x - m) / torch.sqrt(v + eps)
def _scatter_add(tgt, idx, src, axis=0):
    return tgt.clone().index_add_(axis, idx, src)
def _segment_sum(src, seg, n):
    out = torch.zeros((n,) + src.shape[1:], dtype=src.dtype)
    return out.index_add_(0, seg, src)
def _pad(x, pw):
    flat = tuple(v for pair in reversed(pw) for v in pair)
    return torch.nn.functional.pad(x, flat)

kernel_name = 'standalone__fused_io_aware_attn__prompt'

def setup_inputs():

    _w = (lambda a: (lambda t: t.to(torch.bfloat16) if t.is_floating_point() else t)(torch.as_tensor(a)))
    B, H, S = 1, 32, 2048
    Dh = max(1, 512 // 32)
    return {
        "q": _w((lambda *s: torch.randn(*s, dtype=torch.bfloat16) * 0.8)(B, H, S, Dh)),
        "k": _w((lambda *s: torch.randn(*s, dtype=torch.bfloat16) * 0.8)(B, H, S, Dh)),
        "v": _w((lambda *s: torch.randn(*s, dtype=torch.bfloat16) * 0.05)(B, H, S, Dh)),
    }

def reference(q, k, v):
    scale = q.shape[-1] ** -0.5
    s = torch.einsum('bhqd,bhkd->bhqk', q, k) * scale
    p = _softmax(s, axis=-1)
    return torch.einsum('bhqk,bhkd->bhqd', p, v)
```

```cpp
#include <hip/hip_runtime.h>
#include <hip/hip_bf16.h>
#include <stdint.h>

typedef __bf16 bf16x8 __attribute__((ext_vector_type(8)));
typedef __bf16 bf16x16 __attribute__((ext_vector_type(16)));
typedef __bf16 bf16x2 __attribute__((ext_vector_type(2)));
typedef short s16x4 __attribute__((ext_vector_type(4)));
typedef float f32x2 __attribute__((ext_vector_type(2)));
typedef float f32x8 __attribute__((ext_vector_type(8)));
typedef float f32x16 __attribute__((ext_vector_type(16)));
typedef unsigned u32x2 __attribute__((ext_vector_type(2)));
typedef unsigned u32x4 __attribute__((ext_vector_type(4)));
typedef __attribute__((address_space(3))) const char* lds_cptr;
typedef __attribute__((address_space(3))) char* lds_ptr;

constexpr int SEQ = 2048, DH = 16, NH = 32;
constexpr int NT = SEQ / 32;
constexpr int LDS_K = 0, LDS_V = 65536, LDS_ONES = 131072, ONES_BYTES = 8192 + 1024;
constexpr int LDS_BYTES = LDS_ONES + ONES_BYTES;
constexpr float QSCALE = 0.25f * 1.4426950408889634f;

#define DI __device__ __forceinline__
#define SBAR() __builtin_amdgcn_sched_barrier(0)

DI void glds16(const void* g, unsigned lds_base) {
  unsigned sv;
  asm volatile("s_mov_b32 %0, m0\n\ts_mov_b32 m0, %2\n\ts_nop 0\n\tglobal_load_lds_dwordx4 %1, off\n\ts_mov_b32 m0, %0"
               : "=&s"(sv) : "v"(g), "s"(lds_base) : "memory");
}
DI unsigned cvtpk(float lo, float hi) { f32x2 v = {lo, hi}; bf16x2 b = __builtin_convertvector(v, bf16x2); return __builtin_bit_cast(unsigned, b); }
DI bf16x8 kread(lds_cptr p) { return *(const __attribute__((address_space(3))) bf16x8*)p; }
DI s16x4 vtr(lds_cptr p) { return __builtin_amdgcn_ds_read_tr16_b64_v4i16((__attribute__((address_space(3))) s16x4*)p); }
DI bf16x8 vread(lds_cptr p) {
  s16x4 a = vtr(p), b = vtr(p + 128);
  return __builtin_bit_cast(bf16x8, __builtin_shufflevector(a, b, 0, 1, 2, 3, 4, 5, 6, 7));
}
#define MFMA(a, b, c) __builtin_amdgcn_mfma_f32_32x32x16_bf16(a, b, c, 0, 0, 0)
DI f32x16 pv(bf16x8 vf, f32x8 p, f32x16 o, int idx) {
  return __builtin_amdgcn_smfmac_f32_32x32x32_bf16(vf, __builtin_bit_cast(bf16x16, p), o, idx, 0, 0);
}
DI f32x8 exp8(f32x16 s, int h) {
  f32x8 r;
#pragma unroll
  for (int i = 0; i < 8; ++i) r[i] = __builtin_amdgcn_exp2f(s[8 * h + i]);
  return r;
}
DI float max16(const f32x16& s) {
  float a = __builtin_fmaxf(s[0], s[1]);
#pragma unroll
  for (int i = 2; i < 16; i += 2) a = __builtin_fmaxf(__builtin_fmaxf(a, s[i]), s[i + 1]);
  return a;
}
DI float xhalf_max(float m) {
  auto rr = __builtin_amdgcn_permlane32_swap(__float_as_uint(m), __float_as_uint(m), false, false);
  return __builtin_fmaxf(__uint_as_float(rr[0]), __uint_as_float(rr[1]));
}

template <bool LAST>
DI void run8(lds_cptr kp, lds_cptr vp, const bf16x8& qf, const f32x16& negm, f32x16& sa, bf16x8& kf, bf16x8& vf0, bf16x8& vf1, f32x16& o, int idx, int waitsel) {
#pragma unroll
  for (int i = 0; i < 8; ++i) {
    if (i == 5) {
      if (waitsel == 1) asm volatile("s_waitcnt vmcnt(10)\n\ts_barrier" ::: "memory");
      else if (waitsel == 2) asm volatile("s_waitcnt vmcnt(0)\n\ts_barrier" ::: "memory");
    }
    f32x16 sb;
    const bool more = !(LAST && i == 7);
    if (more) sb = MFMA(kf, qf, negm);
    const bool more2 = !(LAST && i >= 6);
    if (more2) kf = kread(kp + (i + 2) * 1024);
    SBAR();
    f32x8 p0 = exp8(sa, 0);
    SBAR();
    o = pv(vf0, p0, o, idx);
    if (more) vf0 = vread(vp + (i + 1) * 1024);
    SBAR();
    f32x8 p1 = exp8(sa, 1);
    SBAR();
    o = pv(vf1, p1, o, idx);
    if (more) vf1 = vread(vp + (i + 1) * 1024 + 512);
    SBAR();
    sa = sb;
  }
}

__global__ __launch_bounds__(512, 2) void attn_fwd(const __hip_bfloat16* __restrict__ q, const __hip_bfloat16* __restrict__ k,
                                                   const __hip_bfloat16* __restrict__ v, __hip_bfloat16* __restrict__ out) {
  __shared__ __attribute__((aligned(1024))) char lds[LDS_BYTES];
  const int tid = threadIdx.x, lane = tid & 63, r32 = lane & 31, hi = lane >> 5;
  const int wid = __builtin_amdgcn_readfirstlane(tid >> 6);
  const int b = blockIdx.x, head = (b & 7) * 4 + (b >> 6), qb = (b >> 3) & 7;
  const unsigned lds0 = (unsigned)(uintptr_t)lds;
  const size_t hoff = (size_t)head * SEQ * DH;
  const int qrow = qb * 256 + wid * 32 + r32;
  u32x4 qraw;
  { const __hip_bfloat16* qp = q + hoff + qrow * DH + hi * 8;
    asm volatile("global_load_dwordx4 %0, %1, off" : "=v"(qraw) : "v"(qp) : "memory"); }
  { const __hip_bfloat16* ks = k + hoff + wid * 512 + r32 * 16 + hi * 8;
    const __hip_bfloat16* vs = v + hoff + wid * 512 + lane * 8;
#pragma unroll
    for (int j = 0; j < 8; ++j) {
      glds16(ks + j * 4096, lds0 + LDS_K + wid * 1024 + j * 8192);
      glds16(vs + j * 4096, lds0 + LDS_V + wid * 1024 + j * 8192);
    } }
  { u32x4 ones = {0x3f803f80u, 0x3f803f80u, 0x3f803f80u, 0x3f803f80u};
    *(__attribute__((address_space(3))) u32x4*)((lds_ptr)lds + LDS_ONES + tid * 16) = ones;
    if (tid < 64) *(__attribute__((address_space(3))) u32x4*)((lds_ptr)lds + LDS_ONES + 8192 + tid * 16) = ones; }
  asm volatile("s_waitcnt vmcnt(16)" : "+v"(qraw) :: "memory");
  bf16x8 qf;
  { u32x4 w;
#pragma unroll
    for (int i = 0; i < 4; ++i) w[i] = cvtpk(__uint_as_float(qraw[i] << 16) * QSCALE, __uint_as_float(qraw[i] & 0xffff0000u) * QSCALE);
    qf = __builtin_bit_cast(bf16x8, w); }
  lds_cptr kp = (lds_cptr)lds + LDS_K + lane * 16;
  const bool ones_lane = (lane >> 4) & 1;
  lds_cptr vp = (lds_cptr)lds + (ones_lane ? LDS_ONES + 128 : LDS_V + hi * 256) + (lane & 15) * 8;
  const int vstep = ones_lane ? 0 : 8192;
  int idx = 0xDDDD; asm volatile("" : "+v"(idx));
  asm volatile("s_waitcnt vmcnt(14) lgkmcnt(0)\n\ts_barrier" ::: "memory");

  bf16x8 kf = kread(kp);
  f32x16 zero = {};
  f32x16 sa = MFMA(kf, qf, zero);
  float m = xhalf_max(max16(sa));
  f32x16 o;
  for (int attempt = 0;; ++attempt) {
    f32x16 negm;
#pragma unroll
    for (int i = 0; i < 16; ++i) negm[i] = -m;
    asm volatile("" : "+v"(negm));
    lds_cptr kq = kp, vq = vp;
    kf = kread(kq);
    sa = MFMA(kf, qf, negm);
    kf = kread(kq + 1024);
    bf16x8 vf0 = vread(vq), vf1 = vread(vq + 512);
    o = zero;
    for (int j = 0; j < 7; ++j) {
      const int ws = attempt ? 0 : (j == 0 ? 1 : (j == 2 ? 2 : 0));
      run8<false>(kq, vq, qf, negm, sa, kf, vf0, vf1, o, idx, ws);
      kq += 8192; vq += vstep;
    }
    run8<true>(kq, vq, qf, negm, sa, kf, vf0, vf1, o, idx, 0);
    float amax = __builtin_fabsf(o[8]);
#pragma unroll
    for (int i = 0; i < 8; ++i) amax = __builtin_fmaxf(amax, __builtin_fabsf(o[i]) * 0x1p-64f);
    bool bad = !(amax < 0x1p60f);
#pragma unroll
    for (int i = 0; i < 9; ++i) bad |= (o[i] != o[i]);
    if (attempt || !__any(bad)) break;
    float mm = -INFINITY;
    for (int t = 0; t < NT; ++t) { f32x16 s = MFMA(kread(kp + t * 1024), qf, zero); mm = __builtin_fmaxf(mm, max16(s)); }
    m = xhalf_max(mm);
  }
  const float rl = 1.0f / o[8];
  unsigned w0 = cvtpk(o[0] * rl, o[1] * rl), w1 = cvtpk(o[2] * rl, o[3] * rl), w2 = cvtpk(o[4] * rl, o[5] * rl), w3 = cvtpk(o[6] * rl, o[7] * rl);
  auto r0 = __builtin_amdgcn_permlane32_swap(w0, w2, false, false);
  auto r1 = __builtin_amdgcn_permlane32_swap(w1, w3, false, false);
  u32x4 res = {r0[0], r1[0], r0[1], r1[1]};
  *(u32x4*)(out + hoff + qrow * DH + hi * 8) = res;
}

extern "C" void kernel_launch(void* const* d_in, const int* in_sizes, int n_in, void* d_out, int out_size, void* d_ws, size_t ws_size, hipStream_t stream) {
  attn_fwd<<<256, 512, 0, stream>>>((const __hip_bfloat16*)d_in[0], (const __hip_bfloat16*)d_in[1], (const __hip_bfloat16*)d_in[2], (__hip_bfloat16*)d_out);
}
```

```cpp
#include <hip/hip_runtime.h>
#include <hip/hip_bf16.h>
#include <stdint.h>

typedef __bf16 bf16x8 __attribute__((ext_vector_type(8)));
typedef __bf16 bf16x16 __attribute__((ext_vector_type(16)));
typedef __bf16 bf16x2 __attribute__((ext_vector_type(2)));
typedef short s16x4 __attribute__((ext_vector_type(4)));
typedef float f32x2 __attribute__((ext_vector_type(2)));
typedef float f32x8 __attribute__((ext_vector_type(8)));
typedef float f32x16 __attribute__((ext_vector_type(16)));
typedef unsigned u32x2 __attribute__((ext_vector_type(2)));
typedef unsigned u32x4 __attribute__((ext_vector_type(4)));
typedef __attribute__((address_space(3))) const char* lds_cptr;
typedef __attribute__((address_space(3))) char* lds_ptr;

constexpr int SEQ = 2048, DH = 16, NH = 32;
constexpr int NT = SEQ / 32;
constexpr int LDS_K = 0, LDS_V = 65536, LDS_ONES = 131072, ONES_BYTES = 8192 + 1024;
constexpr int LDS_BYTES = LDS_ONES + ONES_BYTES;
constexpr float QSCALE = 0.25f * 1.4426950408889634f;

#define DI __device__ __forceinline__
#define SBAR() __builtin_amdgcn_sched_barrier(0)

DI void glds16(const void* g, unsigned lds_base) {
  unsigned sv;
  asm volatile("s_mov_b32 %0, m0\n\ts_mov_b32 m0, %2\n\ts_nop 0\n\tglobal_load_lds_dwordx4 %1, off\n\ts_mov_b32 m0, %0"
               : "=&s"(sv) : "v"(g), "s"(lds_base) : "memory");
}
DI unsigned cvtpk(float lo, float hi) { f32x2 v = {lo, hi}; bf16x2 b = __builtin_convertvector(v, bf16x2); return __builtin_bit_cast(unsigned, b); }
DI bf16x8 kread(lds_cptr p) { return *(const __attribute__((address_space(3))) bf16x8*)p; }
DI s16x4 vtr(lds_cptr p) { return __builtin_amdgcn_ds_read_tr16_b64_v4i16((__attribute__((address_space(3))) s16x4*)p); }
DI bf16x8 vread(lds_cptr p) {
  s16x4 a = vtr(p), b = vtr(p + 128);
  return __builtin_bit_cast(bf16x8, __builtin_shufflevector(a, b, 0, 1, 2, 3, 4, 5, 6, 7));
}
#define MFMA(a, b, c) __builtin_amdgcn_mfma_f32_32x32x16_bf16(a, b, c, 0, 0, 0)
DI f32x16 pv(bf16x8 vf, f32x8 p, f32x16 o, int idx) {
  return __builtin_amdgcn_smfmac_f32_32x32x32_bf16(vf, __builtin_bit_cast(bf16x16, p), o, idx, 0, 0);
}
DI f32x8 exp8(f32x16 s, int h) {
  f32x8 r;
#pragma unroll
  for (int i = 0; i < 8; ++i) r[i] = __builtin_amdgcn_exp2f(s[8 * h + i]);
  return r;
}
DI float max16(const f32x16& s) {
  float a = __builtin_fmaxf(s[0], s[1]);
#pragma unroll
  for (int i = 2; i < 16; i += 2) a = __builtin_fmaxf(__builtin_fmaxf(a, s[i]), s[i + 1]);
  return a;
}
DI float xhalf_max(float m) {
  auto rr = __builtin_amdgcn_permlane32_swap(__float_as_uint(m), __float_as_uint(m), false, false);
  return __builtin_fmaxf(__uint_as_float(rr[0]), __uint_as_float(rr[1]));
}

template <bool LAST>
DI void run8(lds_cptr kp, lds_cptr vp, const bf16x8& qf, const f32x16& negm, f32x16& sa, bf16x8& kf, bf16x8& vf0, bf16x8& vf1, f32x16& o, int idx, int waitsel) {
#pragma unroll
  for (int i = 0; i < 8; ++i) {
    if (i == 5) {
      if (waitsel == 1) asm volatile("s_waitcnt vmcnt(10)\n\ts_barrier" ::: "memory");
      else if (waitsel == 2) asm volatile("s_waitcnt vmcnt(0)\n\ts_barrier" ::: "memory");
    }
    f32x16 sb;
    const bool more = !(LAST && i == 7);
    if (more) sb = MFMA(kf, qf, negm);
    const bool more2 = !(LAST && i >= 6);
    if (more2) kf = kread(kp + (i + 2) * 1024);
    SBAR();
    f32x8 p0 = exp8(sa, 0);
    SBAR();
    o = pv(vf0, p0, o, idx);
    if (more) vf0 = vread(vp + (i + 1) * 1024);
    SBAR();
    f32x8 p1 = exp8(sa, 1);
    SBAR();
    o = pv(vf1, p1, o, idx);
    if (more) vf1 = vread(vp + (i + 1) * 1024 + 512);
    SBAR();
    sa = sb;
  }
}

__global__ __launch_bounds__(512, 2) void attn_fwd(const __hip_bfloat16* __restrict__ q, const __hip_bfloat16* __restrict__ k,
                                                   const __hip_bfloat16* __restrict__ v, __hip_bfloat16* __restrict__ out) {
  unsigned long long st[8];
#define STAMP(i) do { SBAR(); asm volatile("s_memrealtime %0\n\ts_waitcnt lgkmcnt(0)" : "=s"(st[i]) :: "memory"); SBAR(); } while (0)
  STAMP(0);
  __shared__ __attribute__((aligned(1024))) char lds[LDS_BYTES];
  const int tid = threadIdx.x, lane = tid & 63, r32 = lane & 31, hi = lane >> 5;
  const int wid = __builtin_amdgcn_readfirstlane(tid >> 6);
  const int b = blockIdx.x, head = (b & 7) * 4 + (b >> 6), qb = (b >> 3) & 7;
  const unsigned lds0 = (unsigned)(uintptr_t)lds;
  const size_t hoff = (size_t)head * SEQ * DH;
  const int qrow = qb * 256 + wid * 32 + r32;
  u32x4 qraw;
  { const __hip_bfloat16* qp = q + hoff + qrow * DH + hi * 8;
    asm volatile("global_load_dwordx4 %0, %1, off" : "=v"(qraw) : "v"(qp) : "memory"); }
  { const __hip_bfloat16* ks = k + hoff + wid * 512 + r32 * 16 + hi * 8;
    const __hip_bfloat16* vs = v + hoff + wid * 512 + lane * 8;
#pragma unroll
    for (int j = 0; j < 8; ++j) {
      glds16(ks + j * 4096, lds0 + LDS_K + wid * 1024 + j * 8192);
      glds16(vs + j * 4096, lds0 + LDS_V + wid * 1024 + j * 8192);
    } }
  { u32x4 ones = {0x3f803f80u, 0x3f803f80u, 0x3f803f80u, 0x3f803f80u};
    *(__attribute__((address_space(3))) u32x4*)((lds_ptr)lds + LDS_ONES + tid * 16) = ones;
    if (tid < 64) *(__attribute__((address_space(3))) u32x4*)((lds_ptr)lds + LDS_ONES + 8192 + tid * 16) = ones; }
  asm volatile("s_waitcnt vmcnt(16)" : "+v"(qraw) :: "memory");
  bf16x8 qf;
  { u32x4 w;
#pragma unroll
    for (int i = 0; i < 4; ++i) w[i] = cvtpk(__uint_as_float(qraw[i] << 16) * QSCALE, __uint_as_float(qraw[i] & 0xffff0000u) * QSCALE);
    qf = __builtin_bit_cast(bf16x8, w); }
  lds_cptr kp = (lds_cptr)lds + LDS_K + lane * 16;
  const bool ones_lane = (lane >> 4) & 1;
  lds_cptr vp = (lds_cptr)lds + (ones_lane ? LDS_ONES + 128 : LDS_V + hi * 256) + (lane & 15) * 8;
  const int vstep = ones_lane ? 0 : 8192;
  int idx = 0xDDDD; asm volatile("" : "+v"(idx));
  asm volatile("s_waitcnt vmcnt(14) lgkmcnt(0)\n\ts_barrier" ::: "memory");

  STAMP(3);
  bf16x8 kf = kread(kp);
  f32x16 zero = {};
  f32x16 sa = MFMA(kf, qf, zero);
  float m = xhalf_max(max16(sa));
  f32x16 o;
  for (int attempt = 0;; ++attempt) {
    f32x16 negm;
#pragma unroll
    for (int i = 0; i < 16; ++i) negm[i] = -m;
    asm volatile("" : "+v"(negm));
    lds_cptr kq = kp, vq = vp;
    kf = kread(kq);
    sa = MFMA(kf, qf, negm);
    kf = kread(kq + 1024);
    bf16x8 vf0 = vread(vq), vf1 = vread(vq + 512);
    o = zero;
    STAMP(4);
    for (int j = 0; j < 7; ++j) {
      const int ws = attempt ? 0 : (j == 0 ? 1 : (j == 2 ? 2 : 0));
      run8<false>(kq, vq, qf, negm, sa, kf, vf0, vf1, o, idx, ws);
      kq += 8192; vq += vstep;
    }
    run8<true>(kq, vq, qf, negm, sa, kf, vf0, vf1, o, idx, 0);
    STAMP(5);
    float amax = __builtin_fabsf(o[8]);
#pragma unroll
    for (int i = 0; i < 8; ++i) amax = __builtin_fmaxf(amax, __builtin_fabsf(o[i]) * 0x1p-64f);
    bool bad = !(amax < 0x1p60f);
#pragma unroll
    for (int i = 0; i < 9; ++i) bad |= (o[i] != o[i]);
    if (attempt || !__any(bad)) break;
    float mm = -INFINITY;
    for (int t = 0; t < NT; ++t) { f32x16 s = MFMA(kread(kp + t * 1024), qf, zero); mm = __builtin_fmaxf(mm, max16(s)); }
    m = xhalf_max(mm);
  }
  const float rl = 1.0f / o[8];
  unsigned w0 = cvtpk(o[0] * rl, o[1] * rl), w1 = cvtpk(o[2] * rl, o[3] * rl), w2 = cvtpk(o[4] * rl, o[5] * rl), w3 = cvtpk(o[6] * rl, o[7] * rl);
  auto r0 = __builtin_amdgcn_permlane32_swap(w0, w2, false, false);
  auto r1 = __builtin_amdgcn_permlane32_swap(w1, w3, false, false);
  u32x4 res = {r0[0], r1[0], r0[1], r1[1]};
  *(u32x4*)(out + hoff + qrow * DH + hi * 8) = res;
  STAMP(6);
  { unsigned long long tend = st[6] + 0 * (st[3] - st[0]); while (__builtin_amdgcn_s_memrealtime() < tend) __builtin_amdgcn_s_sleep(1); }
}

extern "C" void kernel_launch(void* const* d_in, const int* in_sizes, int n_in, void* d_out, int out_size, void* d_ws, size_t ws_size, hipStream_t stream) {
  attn_fwd<<<256, 512, 0, stream>>>((const __hip_bfloat16*)d_in[0], (const __hip_bfloat16*)d_in[1], (const __hip_bfloat16*)d_in[2], (__hip_bfloat16*)d_out);
}
```

```cpp
#include <hip/hip_runtime.h>
#include <hip/hip_bf16.h>
#include <stdint.h>

typedef __bf16 bf16x8 __attribute__((ext_vector_type(8)));
typedef __bf16 bf16x16 __attribute__((ext_vector_type(16)));
typedef __bf16 bf16x2 __attribute__((ext_vector_type(2)));
typedef short s16x4 __attribute__((ext_vector_type(4)));
typedef float f32x2 __attribute__((ext_vector_type(2)));
typedef float f32x8 __attribute__((ext_vector_type(8)));
typedef float f32x16 __attribute__((ext_vector_type(16)));
typedef unsigned u32x2 __attribute__((ext_vector_type(2)));
typedef unsigned u32x4 __attribute__((ext_vector_type(4)));
typedef __attribute__((address_space(3))) const char* lds_cptr;
typedef __attribute__((address_space(3))) char* lds_ptr;

constexpr int SEQ = 2048, DH = 16, NH = 32;
constexpr int NT = SEQ / 32;
constexpr int LDS_K = 0, LDS_V = 65536, LDS_ONES = 131072, ONES_BYTES = 8192 + 1024;
constexpr int LDS_BYTES = LDS_ONES + ONES_BYTES;
constexpr float QSCALE = 0.25f * 1.4426950408889634f;

#define DI __device__ __forceinline__
#define SBAR() __builtin_amdgcn_sched_barrier(0)

DI void glds16(const void* g, unsigned lds_base) {
  unsigned sv;
  asm volatile("s_mov_b32 %0, m0\n\ts_mov_b32 m0, %2\n\ts_nop 0\n\tglobal_load_lds_dwordx4 %1, off\n\ts_mov_b32 m0, %0"
               : "=&s"(sv) : "v"(g), "s"(lds_base) : "memory");
}
DI unsigned cvtpk(float lo, float hi) { f32x2 v = {lo, hi}; bf16x2 b = __builtin_convertvector(v, bf16x2); return __builtin_bit_cast(unsigned, b); }
DI bf16x8 kread(lds_cptr p) { return *(const __attribute__((address_space(3))) bf16x8*)p; }
DI s16x4 vtr(lds_cptr p) { return __builtin_amdgcn_ds_read_tr16_b64_v4i16((__attribute__((address_space(3))) s16x4*)p); }
DI bf16x8 vread(lds_cptr p) {
  s16x4 a = vtr(p), b = vtr(p + 128);
  return __builtin_bit_cast(bf16x8, __builtin_shufflevector(a, b, 0, 1, 2, 3, 4, 5, 6, 7));
}
#define MFMA(a, b, c) __builtin_amdgcn_mfma_f32_32x32x16_bf16(a, b, c, 0, 0, 0)
DI f32x16 pv(bf16x8 vf, f32x8 p, f32x16 o, int idx) {
  return __builtin_amdgcn_smfmac_f32_32x32x32_bf16(vf, __builtin_bit_cast(bf16x16, p), o, idx, 0, 0);
}
DI f32x8 exp8(f32x16 s, int h) {
  f32x8 r;
#pragma unroll
  for (int i = 0; i < 8; ++i) r[i] = __builtin_amdgcn_exp2f(s[8 * h + i]);
  return r;
}
DI float max16(const f32x16& s) {
  float a = __builtin_fmaxf(s[0], s[1]);
#pragma unroll
  for (int i = 2; i < 16; i += 2) a = __builtin_fmaxf(__builtin_fmaxf(a, s[i]), s[i + 1]);
  return a;
}
DI float xhalf_max(float m) {
  auto rr = __builtin_amdgcn_permlane32_swap(__float_as_uint(m), __float_as_uint(m), false, false);
  return __builtin_fmaxf(__uint_as_float(rr[0]), __uint_as_float(rr[1]));
}

template <bool LAST>
DI void run8(lds_cptr kp, lds_cptr vp, const bf16x8& qf, const f32x16& negm, f32x16& sa, bf16x8& kf, bf16x8& vf0, bf16x8& vf1, f32x16& o, int idx, int waitsel) {
#pragma unroll
  for (int i = 0; i < 8; ++i) {
    if (i == 5) {
      if (waitsel == 1) asm volatile("s_waitcnt vmcnt(10)\n\ts_barrier" ::: "memory");
      else if (waitsel == 2) asm volatile("s_waitcnt vmcnt(0)\n\ts_barrier" ::: "memory");
    }
    f32x16 sb;
    const bool more = !(LAST && i == 7);
    if (more) sb = MFMA(kf, qf, negm);
    const bool more2 = !(LAST && i >= 6);
    if (more2) kf = kread(kp + (i + 2) * 1024);
    SBAR();
    f32x8 p0 = exp8(sa, 0);
    SBAR();
    o = pv(vf0, p0, o, idx);
    if (more) vf0 = vread(vp + (i + 1) * 1024);
    SBAR();
    f32x8 p1 = exp8(sa, 1);
    SBAR();
    o = pv(vf1, p1, o, idx);
    if (more) vf1 = vread(vp + (i + 1) * 1024 + 512);
    SBAR();
    sa = sb;
  }
}

__global__ __launch_bounds__(512, 2) void attn_fwd(const __hip_bfloat16* __restrict__ q, const __hip_bfloat16* __restrict__ k,
                                                   const __hip_bfloat16* __restrict__ v, __hip_bfloat16* __restrict__ out) {
  __shared__ __attribute__((aligned(1024))) char lds[LDS_BYTES];
  const int tid = threadIdx.x, lane = tid & 63, r32 = lane & 31, hi = lane >> 5;
  const int wid = __builtin_amdgcn_readfirstlane(tid >> 6);
  const int b = blockIdx.x, head = (b & 7) * 4 + (b >> 6), qb = (b >> 3) & 7;
  const unsigned lds0 = (unsigned)(uintptr_t)lds;
  const size_t hoff = (size_t)head * SEQ * DH;
  const int qrow = qb * 256 + wid * 32 + r32;
  u32x4 qraw;
  { const __hip_bfloat16* qp = q + hoff + qrow * DH + hi * 8;
    asm volatile("global_load_dwordx4 %0, %1, off" : "=v"(qraw) : "v"(qp) : "memory"); }
  { const __hip_bfloat16* ks = k + hoff + wid * 512 + r32 * 16 + hi * 8;
    const __hip_bfloat16* vs = v + hoff + wid * 512 + lane * 8;
#pragma unroll
    for (int j = 0; j < 8; ++j) {
      glds16(ks + j * 4096, lds0 + LDS_K + wid * 1024 + j * 8192);
      glds16(vs + j * 4096, lds0 + LDS_V + wid * 1024 + j * 8192);
    } }
  { u32x4 ones = {0x3f803f80u, 0x3f803f80u, 0x3f803f80u, 0x3f803f80u};
    *(__attribute__((address_space(3))) u32x4*)((lds_ptr)lds + LDS_ONES + tid * 16) = ones;
    if (tid < 64) *(__attribute__((address_space(3))) u32x4*)((lds_ptr)lds + LDS_ONES + 8192 + tid * 16) = ones; }
  asm volatile("s_waitcnt vmcnt(16)" : "+v"(qraw) :: "memory");
  bf16x8 qf;
  { u32x4 w;
#pragma unroll
    for (int i = 0; i < 4; ++i) w[i] = cvtpk(__uint_as_float(qraw[i] << 16) * QSCALE, __uint_as_float(qraw[i] & 0xffff0000u) * QSCALE);
    qf = __builtin_bit_cast(bf16x8, w); }
  lds_cptr kp = (lds_cptr)lds + LDS_K + lane * 16;
  const bool ones_lane = (lane >> 4) & 1;
  lds_cptr vp = (lds_cptr)lds + (ones_lane ? LDS_ONES + 128 : LDS_V + hi * 256) + (lane & 15) * 8;
  const int vstep = ones_lane ? 0 : 8192;
  int idx = 0xDDDD; asm volatile("" : "+v"(idx));
  asm volatile("s_waitcnt vmcnt(14) lgkmcnt(0)\n\ts_barrier" ::: "memory");

  bf16x8 kf = kread(kp);
  f32x16 zero = {};
  f32x16 sa = MFMA(kf, qf, zero);
  float m = xhalf_max(max16(sa));
  f32x16 o;
  for (int attempt = 0;; ++attempt) {
    f32x16 negm;
#pragma unroll
    for (int i = 0; i < 16; ++i) negm[i] = -m;
    asm volatile("" : "+v"(negm));
    lds_cptr kq = kp, vq = vp;
    kf = kread(kq);
    sa = MFMA(kf, qf, negm);
    kf = kread(kq + 1024);
    bf16x8 vf0 = vread(vq), vf1 = vread(vq + 512);
    o = zero;
    for (int j = 0; j < 7; ++j) {
      const int ws = attempt ? 0 : (j == 0 ? 1 : (j == 2 ? 2 : 0));
      run8<false>(kq, vq, qf, negm, sa, kf, vf0, vf1, o, idx, ws);
      kq += 8192; vq += vstep;
    }
    run8<true>(kq, vq, qf, negm, sa, kf, vf0, vf1, o, idx, 0);
    float amax = __builtin_fabsf(o[8]);
#pragma unroll
    for (int i = 0; i < 8; ++i) amax = __builtin_fmaxf(amax, __builtin_fabsf(o[i]) * 0x1p-64f);
    bool bad = !(amax < 0x1p60f);
#pragma unroll
    for (int i = 0; i < 9; ++i) bad |= (o[i] != o[i]);
    if (attempt || !__any(bad)) break;
    float mm = -INFINITY;
    for (int t = 0; t < NT; ++t) { f32x16 s = MFMA(kread(kp + t * 1024), qf, zero); mm = __builtin_fmaxf(mm, max16(s)); }
    m = xhalf_max(mm);
  }
  const float rl = 1.0f / o[8];
  unsigned w0 = cvtpk(o[0] * rl, o[1] * rl), w1 = cvtpk(o[2] * rl, o[3] * rl), w2 = cvtpk(o[4] * rl, o[5] * rl), w3 = cvtpk(o[6] * rl, o[7] * rl);
  auto r0 = __builtin_amdgcn_permlane32_swap(w0, w2, false, false);
  auto r1 = __builtin_amdgcn_permlane32_swap(w1, w3, false, false);
  u32x4 res = {r0[0], r1[0], r0[1], r1[1]};
  { __hip_bfloat16* op = out + hoff + qrow * DH + hi * 8;
    asm volatile("global_store_dwordx4 %0, %1, off sc1\n\ts_nop 1" :: "v"(op), "v"(res) : "memory"); }
}

extern "C" void kernel_launch(void* const* d_in, const int* in_sizes, int n_in, void* d_out, int out_size, void* d_ws, size_t ws_size, hipStream_t stream) {
  attn_fwd<<<256, 512, 0, stream>>>((const __hip_bfloat16*)d_in[0], (const __hip_bfloat16*)d_in[1], (const __hip_bfloat16*)d_in[2], (__hip_bfloat16*)d_out);
}
```

```cpp
#include <hip/hip_runtime.h>
#include <hip/hip_bf16.h>
#include <stdint.h>

typedef __bf16 bf16x8 __attribute__((ext_vector_type(8)));
typedef __bf16 bf16x16 __attribute__((ext_vector_type(16)));
typedef __bf16 bf16x2 __attribute__((ext_vector_type(2)));
typedef short s16x4 __attribute__((ext_vector_type(4)));
typedef float f32x2 __attribute__((ext_vector_type(2)));
typedef float f32x8 __attribute__((ext_vector_type(8)));
typedef float f32x16 __attribute__((ext_vector_type(16)));
typedef unsigned u32x2 __attribute__((ext_vector_type(2)));
typedef unsigned u32x4 __attribute__((ext_vector_type(4)));
typedef __attribute__((address_space(3))) const char* lds_cptr;
typedef __attribute__((address_space(3))) char* lds_ptr;

constexpr int SEQ = 2048, DH = 16, NH = 32;
constexpr int NT = SEQ / 32;
constexpr int LDS_K = 0, LDS_V = 65536, LDS_ONES = 131072, ONES_BYTES = 8192 + 1024;
constexpr int LDS_BYTES = LDS_ONES + ONES_BYTES;
constexpr float QSCALE = 0.25f * 1.4426950408889634f;

#define DI __device__ __forceinline__
#define SBAR() __builtin_amdgcn_sched_barrier(0)

DI void glds16(const void* g, unsigned lds_base) {
  unsigned sv;
  asm volatile("s_mov_b32 %0, m0\n\ts_mov_b32 m0, %2\n\ts_nop 0\n\tglobal_load_lds_dwordx4 %1, off\n\ts_mov_b32 m0, %0"
               : "=&s"(sv) : "v"(g), "s"(lds_base) : "memory");
}
DI unsigned cvtpk(float lo, float hi) { f32x2 v = {lo, hi}; bf16x2 b = __builtin_convertvector(v, bf16x2); return __builtin_bit_cast(unsigned, b); }
DI bf16x8 kread(lds_cptr p) { return *(const __attribute__((address_space(3))) bf16x8*)p; }
DI s16x4 vtr(lds_cptr p) { return __builtin_amdgcn_ds_read_tr16_b64_v4i16((__attribute__((address_space(3))) s16x4*)p); }
DI bf16x8 vread(lds_cptr p) {
  s16x4 a = vtr(p), b = vtr(p + 128);
  return __builtin_bit_cast(bf16x8, __builtin_shufflevector(a, b, 0, 1, 2, 3, 4, 5, 6, 7));
}
#define MFMA(a, b, c) __builtin_amdgcn_mfma_f32_32x32x16_bf16(a, b, c, 0, 0, 0)
DI f32x16 pv(bf16x8 vf, f32x8 p, f32x16 o, int idx) {
  return __builtin_amdgcn_smfmac_f32_32x32x32_bf16(vf, __builtin_bit_cast(bf16x16, p), o, idx, 0, 0);
}
DI f32x8 exp8(f32x16 s, int h) {
  f32x8 r;
#pragma unroll
  for (int i = 0; i < 8; ++i) r[i] = __builtin_amdgcn_exp2f(s[8 * h + i]);
  return r;
}
DI float max16(const f32x16& s) {
  float a = __builtin_fmaxf(s[0], s[1]);
#pragma unroll
  for (int i = 2; i < 16; i += 2) a = __builtin_fmaxf(__builtin_fmaxf(a, s[i]), s[i + 1]);
  return a;
}
DI float xhalf_max(float m) {
  auto rr = __builtin_amdgcn_permlane32_swap(__float_as_uint(m), __float_as_uint(m), false, false);
  return __builtin_fmaxf(__uint_as_float(rr[0]), __uint_as_float(rr[1]));
}

template <bool LAST>
DI void run8(lds_cptr kp, lds_cptr vp, const bf16x8& qf, const f32x16& negm, f32x16& sa, bf16x8& kf, bf16x8& vf0, bf16x8& vf1, f32x16& o, int idx, int waitsel) {
#pragma unroll
  for (int i = 0; i < 8; ++i) {
    if (i == 5) {
      if (waitsel == 1) asm volatile("s_waitcnt vmcnt(10)\n\ts_barrier" ::: "memory");
      else if (waitsel == 2) asm volatile("s_waitcnt vmcnt(0)\n\ts_barrier" ::: "memory");
    }
    f32x16 sb;
    const bool more = !(LAST && i == 7);
    if (more) sb = MFMA(kf, qf, negm);
    const bool more2 = !(LAST && i >= 6);
    if (more2) kf = kread(kp + (i + 2) * 1024);
    SBAR();
    f32x8 p0 = exp8(sa, 0);
    SBAR();
    o = pv(vf0, p0, o, idx);
    if (more) vf0 = vread(vp + (i + 1) * 1024);
    SBAR();
    f32x8 p1 = exp8(sa, 1);
    SBAR();
    o = pv(vf1, p1, o, idx);
    if (more) vf1 = vread(vp + (i + 1) * 1024 + 512);
    SBAR();
    sa = sb;
  }
}

__global__ __launch_bounds__(512, 2) void attn_fwd(const __hip_bfloat16* __restrict__ q, const __hip_bfloat16* __restrict__ k,
                                                   const __hip_bfloat16* __restrict__ v, __hip_bfloat16* __restrict__ out, unsigned long long* ws) {
  unsigned long long st[8];
#define STAMP(i) do { SBAR(); asm volatile("s_memrealtime %0\n\ts_waitcnt lgkmcnt(0)" : "=s"(st[i]) :: "memory"); SBAR(); } while (0)
  STAMP(0);
  __shared__ __attribute__((aligned(1024))) char lds[LDS_BYTES];
  const int tid = threadIdx.x, lane = tid & 63, r32 = lane & 31, hi = lane >> 5;
  const int wid = __builtin_amdgcn_readfirstlane(tid >> 6);
  const int b = blockIdx.x, head = (b & 7) * 4 + (b >> 6), qb = (b >> 3) & 7;
  const unsigned lds0 = (unsigned)(uintptr_t)lds;
  const size_t hoff = (size_t)head * SEQ * DH;
  const int qrow = qb * 256 + wid * 32 + r32;
  u32x4 qraw;
  { const __hip_bfloat16* qp = q + hoff + qrow * DH + hi * 8;
    asm volatile("global_load_dwordx4 %0, %1, off" : "=v"(qraw) : "v"(qp) : "memory"); }
  { const __hip_bfloat16* ks = k + hoff + wid * 512 + r32 * 16 + hi * 8;
    const __hip_bfloat16* vs = v + hoff + wid * 512 + lane * 8;
#pragma unroll
    for (int j = 0; j < 8; ++j) {
      glds16(ks + j * 4096, lds0 + LDS_K + wid * 1024 + j * 8192);
      glds16(vs + j * 4096, lds0 + LDS_V + wid * 1024 + j * 8192);
    } }
  { u32x4 ones = {0x3f803f80u, 0x3f803f80u, 0x3f803f80u, 0x3f803f80u};
    *(__attribute__((address_space(3))) u32x4*)((lds_ptr)lds + LDS_ONES + tid * 16) = ones;
    if (tid < 64) *(__attribute__((address_space(3))) u32x4*)((lds_ptr)lds + LDS_ONES + 8192 + tid * 16) = ones; }
  asm volatile("s_waitcnt vmcnt(16)" : "+v"(qraw) :: "memory");
  bf16x8 qf;
  { u32x4 w;
#pragma unroll
    for (int i = 0; i < 4; ++i) w[i] = cvtpk(__uint_as_float(qraw[i] << 16) * QSCALE, __uint_as_float(qraw[i] & 0xffff0000u) * QSCALE);
    qf = __builtin_bit_cast(bf16x8, w); }
  lds_cptr kp = (lds_cptr)lds + LDS_K + lane * 16;
  const bool ones_lane = (lane >> 4) & 1;
  lds_cptr vp = (lds_cptr)lds + (ones_lane ? LDS_ONES + 128 : LDS_V + hi * 256) + (lane & 15) * 8;
  const int vstep = ones_lane ? 0 : 8192;
  int idx = 0xDDDD; asm volatile("" : "+v"(idx));
  asm volatile("s_waitcnt vmcnt(14) lgkmcnt(0)\n\ts_barrier" ::: "memory");

  if (tid == 0) __hip_atomic_store(ws + b, st[0], __ATOMIC_RELAXED, __HIP_MEMORY_SCOPE_AGENT);
  bf16x8 kf = kread(kp);
  f32x16 zero = {};
  f32x16 sa = MFMA(kf, qf, zero);
  float m = xhalf_max(max16(sa));
  f32x16 o;
  for (int attempt = 0;; ++attempt) {
    f32x16 negm;
#pragma unroll
    for (int i = 0; i < 16; ++i) negm[i] = -m;
    asm volatile("" : "+v"(negm));
    lds_cptr kq = kp, vq = vp;
    kf = kread(kq);
    sa = MFMA(kf, qf, negm);
    kf = kread(kq + 1024);
    bf16x8 vf0 = vread(vq), vf1 = vread(vq + 512);
    o = zero;
    for (int j = 0; j < 7; ++j) {
      const int ws = attempt ? 0 : (j == 0 ? 1 : (j == 2 ? 2 : 0));
      run8<false>(kq, vq, qf, negm, sa, kf, vf0, vf1, o, idx, ws);
      kq += 8192; vq += vstep;
    }
    run8<true>(kq, vq, qf, negm, sa, kf, vf0, vf1, o, idx, 0);
    float amax = __builtin_fabsf(o[8]);
#pragma unroll
    for (int i = 0; i < 8; ++i) amax = __builtin_fmaxf(amax, __builtin_fabsf(o[i]) * 0x1p-64f);
    bool bad = !(amax < 0x1p60f);
#pragma unroll
    for (int i = 0; i < 9; ++i) bad |= (o[i] != o[i]);
    if (attempt || !__any(bad)) break;
    float mm = -INFINITY;
    for (int t = 0; t < NT; ++t) { f32x16 s = MFMA(kread(kp + t * 1024), qf, zero); mm = __builtin_fmaxf(mm, max16(s)); }
    m = xhalf_max(mm);
  }
  const float rl = 1.0f / o[8];
  unsigned w0 = cvtpk(o[0] * rl, o[1] * rl), w1 = cvtpk(o[2] * rl, o[3] * rl), w2 = cvtpk(o[4] * rl, o[5] * rl), w3 = cvtpk(o[6] * rl, o[7] * rl);
  auto r0 = __builtin_amdgcn_permlane32_swap(w0, w2, false, false);
  auto r1 = __builtin_amdgcn_permlane32_swap(w1, w3, false, false);
  u32x4 res = {r0[0], r1[0], r0[1], r1[1]};
  *(u32x4*)(out + hoff + qrow * DH + hi * 8) = res;
  STAMP(6);
  { unsigned long long mn = st[0];
    for (int i = 0; i < 4; ++i) { unsigned long long x = __hip_atomic_load(ws + i * 64 + lane, __ATOMIC_RELAXED, __HIP_MEMORY_SCOPE_AGENT); if (x < st[0] && st[0] - x < 1200) mn = x < mn ? x : mn; }
    for (int o = 32; o > 0; o >>= 1) { unsigned long long y = __shfl_xor(mn, o); mn = y < mn ? y : mn; }
    unsigned long long tend = st[6] + (0 * (st[0] - mn)); while (__builtin_amdgcn_s_memrealtime() < tend) __builtin_amdgcn_s_sleep(1); }
}

extern "C" void kernel_launch(void* const* d_in, const int* in_sizes, int n_in, void* d_out, int out_size, void* d_ws, size_t ws_size, hipStream_t stream) {
  attn_fwd<<<256, 512, 0, stream>>>((const __hip_bfloat16*)d_in[0], (const __hip_bfloat16*)d_in[1], (const __hip_bfloat16*)d_in[2], (__hip_bfloat16*)d_out, (unsigned long long*)d_ws);
}
```

```cpp
#include <hip/hip_runtime.h>
#include <hip/hip_bf16.h>
#include <stdint.h>

typedef __bf16 bf16x8 __attribute__((ext_vector_type(8)));
typedef __bf16 bf16x16 __attribute__((ext_vector_type(16)));
typedef __bf16 bf16x2 __attribute__((ext_vector_type(2)));
typedef short s16x4 __attribute__((ext_vector_type(4)));
typedef float f32x2 __attribute__((ext_vector_type(2)));
typedef float f32x8 __attribute__((ext_vector_type(8)));
typedef float f32x16 __attribute__((ext_vector_type(16)));
typedef unsigned u32x4 __attribute__((ext_vector_type(4)));
#define AS3 __attribute__((address_space(3)))
typedef AS3 const char* lds_cptr;
typedef AS3 char* lds_ptr;

constexpr int SEQ = 2048, DH = 16;
constexpr int NT = SEQ / 32;
constexpr int LDS_K = 0, LDS_V = 65536, LDS_ONES = 131072, ONES_BYTES = 9216, LDS_FLAG = LDS_ONES + ONES_BYTES;
constexpr int LDS_BYTES = LDS_FLAG + 64;
constexpr float QSCALE = 0.25f * 1.4426950408889634f;

#define DI __device__ __forceinline__
#define SBAR() __builtin_amdgcn_sched_barrier(0)
#define PIN(x) asm volatile("" : "+v"(x))
#define STAMP(i)

DI void glds16(const void* sbase, unsigned voff, unsigned lds_base) {
  asm volatile("s_mov_b32 m0, %2\n\ts_nop 0\n\tglobal_load_lds_dwordx4 %0, %1" :: "v"(voff), "s"(sbase), "s"(lds_base) : "memory");
}
DI unsigned cvtpk(float lo, float hi) { f32x2 v = {lo, hi}; bf16x2 b = __builtin_convertvector(v, bf16x2); return __builtin_bit_cast(unsigned, b); }
DI bf16x8 kread(lds_cptr p) { return *(const AS3 bf16x8*)p; }
DI s16x4 vtr(lds_cptr p) { return __builtin_amdgcn_ds_read_tr16_b64_v4i16((AS3 s16x4*)p); }
DI bf16x8 vread(lds_cptr p) {
  s16x4 a = vtr(p), b = vtr(p + 128);
  return __builtin_bit_cast(bf16x8, __builtin_shufflevector(a, b, 0, 1, 2, 3, 4, 5, 6, 7));
}
#define MFMA(a, b, c) __builtin_amdgcn_mfma_f32_32x32x16_bf16(a, b, c, 0, 0, 0)
DI f32x16 pv(bf16x8 vf, f32x8 p, f32x16 o, int idx) {
  return __builtin_amdgcn_smfmac_f32_32x32x32_bf16(vf, __builtin_bit_cast(bf16x16, p), o, idx, 0, 0);
}
DI float max16(const f32x16& s) {
  float a = __builtin_fmaxf(s[0], s[1]);
#pragma unroll
  for (int i = 2; i < 16; i += 2) a = __builtin_fmaxf(__builtin_fmaxf(a, s[i]), s[i + 1]);
  return a;
}
DI float xhalf_max(float m) {
  auto rr = __builtin_amdgcn_permlane32_swap(__float_as_uint(m), __float_as_uint(m), false, false);
  return __builtin_fmaxf(__uint_as_float(rr[0]), __uint_as_float(rr[1]));
}
#define EX(x) __builtin_amdgcn_exp2f(x)

#define S_(x) #x
#define STR(x) S_(x)
#define EXP1(A, i) "v_exp_f32 v[" STR(A) "+" STR(i) "], v[" STR(A) "+" STR(i) "]\n\t"
#define SMF(VA, P) "v_smfmac_f32_32x32x32_bf16 v[0:15], v[" STR(VA) ":" STR(VA) "+3], v[" STR(P) ":" STR(P) "+7], v84\n\t"
#define QK(B) "v_mfma_f32_32x32x16_bf16 v[" STR(B) ":" STR(B) "+15], v[64:67], v[80:83], v[16:31]\n\t"
#define VRD(R, off) "ds_read_b64_tr_b16 v[" STR(R) ":" STR(R) "+1], v86 offset:" STR(off) "\n\tds_read_b64_tr_b16 v[" STR(R) "+2:" STR(R) "+3], v86 offset:" STR(off) "+128\n\t"
#define KRD(off) "ds_read_b128 v[64:67], v85 offset:" STR(off) "\n\t"
#define TILE_HEAD(A, B) EXP1(A, 0) EXP1(A, 1) "s_waitcnt lgkmcnt(0)\n\t" SMF(76, B + 8)
#define TILE_REST(A, B, VC, VN, i) VRD(76, (i) * 1024 + 512) EXP1(A, 2) EXP1(A, 3) EXP1(A, 4) EXP1(A, 5) QK(B) KRD(((i) + 2) * 1024) VRD(VN, ((i) + 1) * 1024) \
  EXP1(A, 6) EXP1(A, 7) EXP1(A, 8) EXP1(A, 9) SMF(VC, A) EXP1(A, 10) EXP1(A, 11) EXP1(A, 12) EXP1(A, 13) EXP1(A, 14) EXP1(A, 15)
#define TILE_E(i) TILE_HEAD(32, 48) TILE_REST(32, 48, 68, 72, i)
#define TILE_O(i) TILE_HEAD(48, 32) TILE_REST(48, 32, 72, 68, i)
#define MAIN_LOOP \
  "s_mov_b32 %[j], 0\n\t" \
  "s_setprio 3\n\t" \
  "s_nop 10\n\t" \
  EXP1(32, 0) EXP1(32, 1) \
  "s_branch 2f\n\t" \
  "1:\n\t" \
  TILE_HEAD(32, 48) \
  "2:\n\t" \
  TILE_REST(32, 48, 68, 72, 0) TILE_O(1) TILE_E(2) TILE_O(3) TILE_E(4) TILE_O(5) \
  "s_cmp_gt_i32 %[rdy], %[j]\n\t" \
  "s_cbranch_scc1 4f\n\t" \
  "3:\n\t" \
  "ds_read_b32 v89, v88\n\t" \
  "s_waitcnt lgkmcnt(0)\n\t" \
  "v_readfirstlane_b32 %[rdy], v89\n\t" \
  "s_nop 3\n\t" \
  "s_cmp_gt_i32 %[rdy], %[j]\n\t" \
  "s_cbranch_scc0 3b\n\t" \
  "4:\n\t" \
  "s_cmp_lg_u32 %[j], 7\n\t" \
  "s_cbranch_scc1 5f\n\t" \
  "s_setprio 0\n\t" \
  "5:\n\t" \
  TILE_E(6) TILE_O(7) \
  "v_add_u32 v85, 0x2000, v85\n\t" \
  "v_add_u32 v86, v87, v86\n\t" \
  "s_add_u32 %[j], %[j], 1\n\t" \
  "s_cmp_lt_u32 %[j], 5\n\t" \
  "s_cbranch_scc1 1b\n\t" \
  "s_setprio 2\n\t" \
  "s_cmp_lt_u32 %[j], 7\n\t" \
  "s_cbranch_scc1 1b\n\t" \
  "s_setprio 1\n\t" \
  "s_cmp_lt_u32 %[j], 8\n\t" \
  "s_cbranch_scc1 1b\n\t" \
  "s_waitcnt lgkmcnt(0)\n\t" \
  SMF(76, 48 + 8) \
  "s_nop 15\n\t"

__global__ __launch_bounds__(576, 2) void attn_fwd(const __hip_bfloat16* __restrict__ q, const __hip_bfloat16* __restrict__ k,
                                                   const __hip_bfloat16* __restrict__ v, __hip_bfloat16* __restrict__ out
                                                   ) {
  __shared__ __attribute__((aligned(1024))) char lds[LDS_BYTES + 2048];
  STAMP(0);
  const int tid = threadIdx.x, lane = tid & 63, r32 = lane & 31, hi = lane >> 5;
  const int wid = __builtin_amdgcn_readfirstlane(tid >> 6);
  const int b = blockIdx.x, head = (b & 7) * 4 + (b >> 6), qb = (b >> 3) & 7;
  const unsigned lds0 = (unsigned)(uintptr_t)lds;
  const size_t hoff = (size_t)head * SEQ * DH;
  const unsigned koff = r32 * 32 + hi * 16, voff = lane * 16;
  volatile AS3 int* flag = (volatile AS3 int*)((lds_ptr)lds + LDS_FLAG);
  if (wid == 8) {
    *flag = 0;
    const char* kb = (const char*)(k + hoff); const char* vb = (const char*)(v + hoff);
    __builtin_amdgcn_s_sleep(2);
#pragma unroll
    for (int t = 8; t < 16; ++t) { glds16(kb + t * 1024, koff, lds0 + LDS_K + t * 1024); glds16(vb + t * 1024, voff, lds0 + LDS_V + t * 1024); }
    asm volatile("s_waitcnt lgkmcnt(0)\n\ts_barrier" ::: "memory");
    for (int c = 2; c < 8; ++c) {
#pragma unroll
      for (int i = 0; i < 8; ++i) { const int t = c * 8 + i; glds16(kb + t * 1024, koff, lds0 + LDS_K + t * 1024); glds16(vb + t * 1024, voff, lds0 + LDS_V + t * 1024); }
      asm volatile("s_waitcnt vmcnt(16)" ::: "memory");
      *flag = c - 1;
    }
    asm volatile("s_waitcnt vmcnt(0)" ::: "memory");
    *flag = 8;
    return;
  }
  const int qrow = qb * 256 + wid * 32 + r32;
  u32x4 qraw;
  asm volatile("global_load_dwordx4 %0, %1, %2" : "=v"(qraw) : "v"(koff), "s"((const char*)(q + hoff) + (qb * 8 + wid) * 1024) : "memory");
  glds16((const char*)(k + hoff) + wid * 1024, koff, lds0 + LDS_K + wid * 1024);
  glds16((const char*)(v + hoff) + wid * 1024, voff, lds0 + LDS_V + wid * 1024);
  STAMP(1);
  { const unsigned one = (tid & 1) ? 0u : 0x3f80u;
    u32x4 ones = {one, 0u, one, 0u};
    *(AS3 u32x4*)((lds_ptr)lds + LDS_ONES + tid * 16) = ones;
    if (tid < 64) *(AS3 u32x4*)((lds_ptr)lds + LDS_ONES + 8192 + tid * 16) = ones; }
  lds_cptr kp = (lds_cptr)lds + LDS_K + lane * 16;
  const bool ones_lane = (lane >> 4) & 1;
  lds_cptr vp = (lds_cptr)lds + (ones_lane ? LDS_ONES + 128 : LDS_V + hi * 256) + (lane & 15) * 8;
  const int vstep = ones_lane ? 0 : 8192;
  int idx = 0xDDDD; PIN(idx);
  f32x16 zero = {};
  asm volatile("s_waitcnt vmcnt(2)" : "+v"(qraw) :: "memory");
  STAMP(2);
  bf16x8 qf;
  { u32x4 w;
#pragma unroll
    for (int i = 0; i < 4; ++i) w[i] = cvtpk(__uint_as_float(qraw[i] << 16) * QSCALE, __uint_as_float(qraw[i] & 0xffff0000u) * QSCALE);
    qf = __builtin_bit_cast(bf16x8, w); PIN(qf); }
  asm volatile("s_waitcnt vmcnt(0) lgkmcnt(0)\n\ts_barrier" ::: "memory");
  STAMP(3);
  bf16x8 k0 = kread(kp);
  float m = xhalf_max(max16(MFMA(k0, qf, zero)));
  int ready = 0;
  f32x16 o;
  for (int attempt = 0;; ++attempt) {
    f32x16 negm;
#pragma unroll
    for (int i = 0; i < 16; ++i) negm[i] = -m;
    bf16x8 kf = kread(kp + 1024), va = vread(vp);
    f32x16 sa = MFMA(k0, qf, negm);
    o = zero;
    unsigned ka = (unsigned)(uintptr_t)kp, vad = (unsigned)(uintptr_t)vp; int j;
    STAMP(4);
    asm volatile(MAIN_LOOP
      : "+{v[0:15]}"(o), "+{v[32:47]}"(sa), "+{v[64:67]}"(kf), "+{v[68:71]}"(va), "+{v85}"(ka), "+{v86}"(vad), [rdy] "+s"(ready), [j] "=&s"(j)
      : "{v[16:31]}"(negm), "{v[80:83]}"(qf), "{v84}"(idx), "{v87}"(vstep), "{v88}"((unsigned)(uintptr_t)flag)
      : "v48", "v49", "v50", "v51", "v52", "v53", "v54", "v55", "v56", "v57", "v58", "v59", "v60", "v61", "v62", "v63", "v72", "v73", "v74", "v75", "v76", "v77", "v78", "v79", "v89", "scc", "memory");
    STAMP(5);
    float chk = o[8];
#pragma unroll
    for (int i = 0; i < 8; ++i) chk = __builtin_fmaf(__builtin_fabsf(o[i]), 0x1p-40f, chk);
    if (attempt || !__any(!(chk < 0x1p60f))) break;
    float mm = -INFINITY;
    for (int t = 0; t < NT; ++t) mm = __builtin_fmaxf(mm, max16(MFMA(kread(kp + t * 1024), qf, zero)));
    m = xhalf_max(mm);
  }
  const float rl = __builtin_amdgcn_rcpf(o[8]);
  unsigned w0 = cvtpk(o[0] * rl, o[1] * rl), w1 = cvtpk(o[2] * rl, o[3] * rl), w2 = cvtpk(o[4] * rl, o[5] * rl), w3 = cvtpk(o[6] * rl, o[7] * rl);
  auto r0 = __builtin_amdgcn_permlane32_swap(w0, w2, false, false);
  auto r1 = __builtin_amdgcn_permlane32_swap(w1, w3, false, false);
  u32x4 res = {r0[0], r1[0], r0[1], r1[1]};
  asm volatile("global_store_dwordx4 %0, %1, %2 sc1\n\ts_nop 1" :: "v"(koff), "v"(res), "s"((char*)(out + hoff) + (qb * 8 + wid) * 1024) : "memory");
  (void)qrow;
}

extern "C" void kernel_launch(void* const* d_in, const int* in_sizes, int n_in, void* d_out, int out_size, void* d_ws, size_t ws_size, hipStream_t stream) {
  attn_fwd<<<256, 576, 0, stream>>>((const __hip_bfloat16*)d_in[0], (const __hip_bfloat16*)d_in[1], (const __hip_bfloat16*)d_in[2], (__hip_bfloat16*)d_out
  );
}
```

```cpp
#include <hip/hip_runtime.h>
#include <hip/hip_bf16.h>
#include <stdint.h>

typedef __bf16 bf16x8 __attribute__((ext_vector_type(8)));
typedef __bf16 bf16x2 __attribute__((ext_vector_type(2)));
typedef short s16x4 __attribute__((ext_vector_type(4)));
typedef float f32x2 __attribute__((ext_vector_type(2)));
typedef float f32x16 __attribute__((ext_vector_type(16)));
typedef unsigned u32x4 __attribute__((ext_vector_type(4)));
#define AS3 __attribute__((address_space(3)))
typedef AS3 const char* lds_cptr;
typedef AS3 char* lds_ptr;

constexpr int SEQ = 2048, DH = 16;
constexpr int NT = SEQ / 32;
constexpr int LDS_K = 0, LDS_V = 65536, LDS_ONES = 131072, ONES_BYTES = 9216, LDS_FLAG = LDS_ONES + ONES_BYTES;
constexpr int LDS_BYTES = LDS_FLAG + 64;
constexpr float QSCALE = 0.25f * 1.4426950408889634f;

#define DI __device__ __forceinline__
#define SBAR() __builtin_amdgcn_sched_barrier(0)
#define PIN(x) asm volatile("" : "+v"(x))
#define STAMP(i)

DI void glds16(const void* sbase, unsigned voff, unsigned lds_base) {
  asm volatile("s_mov_b32 m0, %2\n\ts_nop 0\n\tglobal_load_lds_dwordx4 %0, %1" :: "v"(voff), "s"(sbase), "s"(lds_base) : "memory");
}
DI unsigned cvtpk(float lo, float hi) { f32x2 v = {lo, hi}; bf16x2 b = __builtin_convertvector(v, bf16x2); return __builtin_bit_cast(unsigned, b); }
DI bf16x8 kread(lds_cptr p) { return *(const AS3 bf16x8*)p; }
DI s16x4 vtr(lds_cptr p) { return __builtin_amdgcn_ds_read_tr16_b64_v4i16((AS3 s16x4*)p); }
DI bf16x8 vread(lds_cptr p) {
  s16x4 a = vtr(p), b = vtr(p + 128);
  return __builtin_bit_cast(bf16x8, __builtin_shufflevector(a, b, 0, 1, 2, 3, 4, 5, 6, 7));
}
#define MFMA(a, b, c) __builtin_amdgcn_mfma_f32_32x32x16_bf16(a, b, c, 0, 0, 0)
DI float max3(float a, float b, float c) { return __builtin_fmaxf(__builtin_fmaxf(a, b), c); }
DI float max16(const f32x16& s) {
  const float a = max3(s[0], s[1], s[2]), b = max3(s[3], s[4], s[5]), c = max3(s[6], s[7], s[8]), d = max3(s[9], s[10], s[11]), e = max3(s[12], s[13], s[14]);
  return __builtin_fmaxf(max3(a, b, c), max3(d, e, s[15]));
}
DI float xhalf_max(float m) {
  auto rr = __builtin_amdgcn_permlane32_swap(__float_as_uint(m), __float_as_uint(m), false, false);
  return __builtin_fmaxf(__uint_as_float(rr[0]), __uint_as_float(rr[1]));
}

#define S_(x) #x
#define STR(x) S_(x)
#define EX(A, i) "v_exp_f32 v[" STR(A) "+" STR(i) "], v[" STR(A) "+" STR(i) "]\n\t"
#define SMF(VA, P) "v_smfmac_f32_32x32x32_bf16 v[0:15], v[" STR(VA) ":" STR(VA) "+3], v[" STR(P) ":" STR(P) "+7], v80\n\t"
#define QK(B) "v_mfma_f32_32x32x16_bf16 v[" STR(B) ":" STR(B) "+15], v[64:67], v[76:79], v[16:31]\n\t"
#define VRD(R, off) "ds_read_b64_tr_b16 v[" STR(R) ":" STR(R) "+1], v82 offset:" STR(off) "\n\tds_read_b64_tr_b16 v[" STR(R) "+2:" STR(R) "+3], v82 offset:" STR(off) "+128\n\t"
#define KRD(off) "ds_read_b128 v[64:67], v81 offset:" STR(off) "\n\t"
#define TILE_HEAD(A, B) EX(A, 0) EX(A, 1) "s_waitcnt lgkmcnt(0)\n\t" SMF(72, B + 8)
#define TILE_REST(A, B, i) VRD(72, (i) * 1024 + 512) EX(A, 2) EX(A, 3) EX(A, 4) EX(A, 5) EX(A, 6) QK(B) KRD(((i) + 2) * 1024) \
  EX(A, 7) EX(A, 8) EX(A, 9) EX(A, 10) EX(A, 11) SMF(68, A) VRD(68, ((i) + 1) * 1024) EX(A, 12) EX(A, 13) EX(A, 14) EX(A, 15)
#define TILE_E(i) TILE_HEAD(32, 48) TILE_REST(32, 48, i)
#define TILE_O(i) TILE_HEAD(48, 32) TILE_REST(48, 32, i)
#define MAIN_LOOP \
  "s_mov_b32 %[j], 0\n\t" \
  "s_setprio 3\n\t" \
  "s_nop 9\n\t" \
  EX(32, 0) EX(32, 1) \
  "s_branch 2f\n\t" \
  "1:\n\t" \
  TILE_HEAD(32, 48) \
  "2:\n\t" \
  TILE_REST(32, 48, 0) TILE_O(1) TILE_E(2) TILE_O(3) TILE_E(4) TILE_O(5) \
  "s_cmp_gt_i32 %[rdy], %[j]\n\t" \
  "s_cbranch_scc1 4f\n\t" \
  "3:\n\t" \
  "ds_read_b32 v85, v84\n\t" \
  "s_waitcnt lgkmcnt(0)\n\t" \
  "v_readfirstlane_b32 %[rdy], v85\n\t" \
  "s_nop 3\n\t" \
  "s_cmp_gt_i32 %[rdy], %[j]\n\t" \
  "s_cbranch_scc0 3b\n\t" \
  "4:\n\t" \
  "s_cmp_lg_u32 %[j], 7\n\t" \
  "s_cbranch_scc1 5f\n\t" \
  "s_setprio 0\n\t" \
  "5:\n\t" \
  TILE_E(6) TILE_O(7) \
  "v_add_u32 v81, 0x2000, v81\n\t" \
  "v_add_u32 v82, v83, v82\n\t" \
  "s_add_u32 %[j], %[j], 1\n\t" \
  "s_cmp_lt_u32 %[j], 5\n\t" \
  "s_cbranch_scc1 1b\n\t" \
  "s_setprio 2\n\t" \
  "s_cmp_lt_u32 %[j], 7\n\t" \
  "s_cbranch_scc1 1b\n\t" \
  "s_setprio 1\n\t" \
  "s_cmp_lt_u32 %[j], 8\n\t" \
  "s_cbranch_scc1 1b\n\t" \
  "s_setprio 3\n\t" \
  "s_waitcnt lgkmcnt(0)\n\t" \
  SMF(72, 48 + 8) \
  "s_nop 11\n\t"

__global__ __launch_bounds__(576, 2) void attn_fwd(const __hip_bfloat16* __restrict__ q, const __hip_bfloat16* __restrict__ k,
                                                   const __hip_bfloat16* __restrict__ v, __hip_bfloat16* __restrict__ out
                                                   ) {
  __shared__ __attribute__((aligned(1024))) char lds[LDS_BYTES];
  STAMP(0);
  const int tid = threadIdx.x, lane = tid & 63, r32 = lane & 31, hi = lane >> 5;
  const int wid = __builtin_amdgcn_readfirstlane(tid >> 6);
  const int b = blockIdx.x, head = (b & 7) * 4 + (b >> 6), qb = (b >> 3) & 7;
  const unsigned lds0 = (unsigned)(uintptr_t)lds;
  const unsigned hoff = head * (SEQ * DH * 2);
  unsigned koff = hoff + wid * 1024 + r32 * 32 + hi * 16, voff = hoff + wid * 1024 + lane * 16;
  unsigned qoff = koff + qb * 8192, aoff = hoff + hi * 16;
  unsigned mk = lds0 + LDS_K + wid * 1024, mv = lds0 + LDS_V + wid * 1024;
  asm volatile("" : "+v"(koff), "+v"(voff), "+v"(qoff), "+v"(aoff), "+s"(mk), "+s"(mv));
  SBAR();
  asm volatile("" : "+s"(q), "+s"(k), "+s"(v), "+s"(out));
  volatile AS3 int* flag = (volatile AS3 int*)((lds_ptr)lds + LDS_FLAG);
  if (wid == 8) {
    *flag = 0;
    const char* kb = (const char*)k; const char* vb = (const char*)v;
    __builtin_amdgcn_s_sleep(2);
#pragma unroll
    for (int i = 0; i < 8; ++i) { glds16(kb + i * 1024, koff, mk + i * 1024); glds16(vb + i * 1024, voff, mv + i * 1024); }
    asm volatile("s_waitcnt lgkmcnt(0)\n\ts_barrier" ::: "memory");
    for (int c = 1; c < 7; ++c) {
#pragma unroll
      for (int i = 0; i < 8; ++i) { glds16(kb + (c * 8 + i) * 1024, koff, mk + (c * 8 + i) * 1024); glds16(vb + (c * 8 + i) * 1024, voff, mv + (c * 8 + i) * 1024); }
      asm volatile("s_waitcnt vmcnt(16)" ::: "memory");
      *flag = c;
    }
    asm volatile("s_waitcnt vmcnt(0)" ::: "memory");
    *flag = 8;
    { unsigned dummy; asm volatile("global_load_dword %0, %1, %2\n\ts_waitcnt vmcnt(0)" : "=v"(dummy) : "v"(hoff + qb * 8192), "s"(out) : "memory"); }
    return;
  }
  u32x4 qraw, araw;
  asm volatile("global_load_dwordx4 %0, %2, %4\n\tglobal_load_dwordx4 %1, %3, %5" : "=&v"(qraw), "=&v"(araw) : "v"(qoff), "v"(aoff), "s"(q), "s"(k) : "memory");
  glds16(k, koff, mk);
  glds16(v, voff, mv);
  STAMP(1);
  { const unsigned one = (tid & 1) ? 0u : 0x3f80u;
    u32x4 ones = {one, 0u, one, 0u};
    *(AS3 u32x4*)((lds_ptr)lds + LDS_ONES + tid * 16) = ones;
    if (tid < 64) *(AS3 u32x4*)((lds_ptr)lds + LDS_ONES + 8192 + tid * 16) = ones; }
  unsigned ka0 = lds0 + LDS_K + lane * 16;
  const bool ones_lane = (lane >> 4) & 1;
  unsigned va0 = lds0 + (ones_lane ? LDS_ONES + 128 : LDS_V + hi * 256) + (lane & 15) * 8;
  int vstep = ones_lane ? 0 : 8192;
  int idx = 0xDDDD;
  unsigned fa = lds0 + LDS_FLAG;
  f32x16 o = {};
  asm volatile("" : "+{v[0:15]}"(o), "+{v80}"(idx), "+{v83}"(vstep), "+{v84}"(fa), "+v"(ka0), "+v"(va0));
  const f32x16 zero = {};
  asm volatile("s_waitcnt vmcnt(2)" : "+v"(qraw), "+v"(araw) :: "memory");
  STAMP(2);
  bf16x8 qf;
  { u32x4 w;
#pragma unroll
    for (int i = 0; i < 4; ++i) w[i] = cvtpk(__uint_as_float(qraw[i] << 16) * QSCALE, __uint_as_float(qraw[i] & 0xffff0000u) * QSCALE);
    qf = __builtin_bit_cast(bf16x8, w); }
  const lds_cptr kp = (lds_cptr)(uintptr_t)ka0, vp = (lds_cptr)(uintptr_t)va0;
  f32x16 negm = MFMA(__builtin_bit_cast(bf16x8, araw ^ 0x80008000u), qf, zero);
  asm volatile("s_waitcnt vmcnt(0) lgkmcnt(0)\n\ts_barrier" ::: "memory");
  STAMP(3);
  int ready = 0;
  for (int attempt = 0;; ++attempt) {
    bf16x8 k0 = kread(kp), kf = kread(kp + 1024), va = vread(vp);
    f32x16 sa = MFMA(k0, qf, negm);
    unsigned ka = ka0, vad = va0; int j;
    STAMP(4);
    asm volatile(MAIN_LOOP
      : "+{v[0:15]}"(o), "+{v[32:47]}"(sa), "+{v[64:67]}"(kf), "+{v[68:71]}"(va), "+{v81}"(ka), "+{v82}"(vad), [rdy] "+s"(ready), [j] "=&s"(j)
      : "{v[16:31]}"(negm), "{v[76:79]}"(qf), "{v80}"(idx), "{v83}"(vstep), "{v84}"(fa)
      : "v48", "v49", "v50", "v51", "v52", "v53", "v54", "v55", "v56", "v57", "v58", "v59", "v60", "v61", "v62", "v63", "v72", "v73", "v74", "v75", "v85", "scc", "memory");
    STAMP(5);
    const float rl = __builtin_amdgcn_rcpf(o[8]);
    unsigned w0 = cvtpk(o[0] * rl, o[1] * rl), w1 = cvtpk(o[2] * rl, o[3] * rl), w2 = cvtpk(o[4] * rl, o[5] * rl), w3 = cvtpk(o[6] * rl, o[7] * rl);
    auto r0 = __builtin_amdgcn_permlane32_swap(w0, w2, false, false);
    auto r1 = __builtin_amdgcn_permlane32_swap(w1, w3, false, false);
    u32x4 res = {r0[0], r1[0], r0[1], r1[1]};
    asm volatile("global_store_dwordx4 %0, %1, %2 sc1\n\ts_nop 1" :: "v"(qoff), "v"(res), "s"(out) : "memory");
    SBAR();
    float chk = o[8];
#pragma unroll
    for (int i = 0; i < 8; ++i) chk = __builtin_fmaf(__builtin_fabsf(o[i]), 0x1p-40f, chk);
    if (attempt || !__any(!(chk < 0x1p60f))) break;
    float mm = -INFINITY;
    for (int t = 0; t < NT; ++t) mm = __builtin_fmaxf(mm, max16(MFMA(kread(kp + t * 1024), qf, zero)));
    const float m = xhalf_max(mm);
#pragma unroll
    for (int i = 0; i < 16; ++i) negm[i] = -m;
    o = zero;
  }
}

extern "C" void kernel_launch(void* const* d_in, const int* in_sizes, int n_in, void* d_out, int out_size, void* d_ws, size_t ws_size, hipStream_t stream) {
  attn_fwd<<<256, 576, 0, stream>>>((const __hip_bfloat16*)d_in[0], (const __hip_bfloat16*)d_in[1], (const __hip_bfloat16*)d_in[2], (__hip_bfloat16*)d_out
  );
}
```

```cpp
#include <hip/hip_runtime.h>
#include <hip/hip_bf16.h>
#include <stdint.h>

typedef __bf16 bf16x8 __attribute__((ext_vector_type(8)));
typedef __bf16 bf16x16 __attribute__((ext_vector_type(16)));
typedef __bf16 bf16x2 __attribute__((ext_vector_type(2)));
typedef short s16x4 __attribute__((ext_vector_type(4)));
typedef float f32x2 __attribute__((ext_vector_type(2)));
typedef float f32x4 __attribute__((ext_vector_type(4)));
typedef float f32x8 __attribute__((ext_vector_type(8)));
typedef float f32x16 __attribute__((ext_vector_type(16)));
typedef unsigned u32x4 __attribute__((ext_vector_type(4)));
typedef unsigned u32x8 __attribute__((ext_vector_type(8)));
#define AS3 __attribute__((address_space(3)))
typedef AS3 const char* lds_cptr;
typedef AS3 char* lds_ptr;

#define LSLEEP 2
#define LPRE 2
constexpr int SEQ = 2048, DH = 16;
constexpr int NT = SEQ / 32;
constexpr int LDS_K = 0, LDS_V = 65536, LDS_ONES = 131072, ONES_BYTES = 5120, LDS_FLAG = LDS_ONES + ONES_BYTES;
constexpr int LDS_EX = LDS_FLAG + 256, EX_SLOT = 2304, LDS_BYTES = LDS_EX + 8 * EX_SLOT;
constexpr float QSCALE = 0.25f * 1.4426950408889634f;

#define DI __device__ __forceinline__
#define SBAR() __builtin_amdgcn_sched_barrier(0)
#define STAMP(i)

DI void glds16(const void* sbase, unsigned voff, unsigned lds_base) {
  asm volatile("s_mov_b32 m0, %2\n\ts_nop 0\n\tglobal_load_lds_dwordx4 %0, %1" :: "v"(voff), "s"(sbase), "s"(lds_base) : "memory");
}
DI unsigned cvtpk(float lo, float hi) { f32x2 v = {lo, hi}; bf16x2 b = __builtin_convertvector(v, bf16x2); return __builtin_bit_cast(unsigned, b); }
DI bf16x8 kread(lds_cptr p) { return *(const AS3 bf16x8*)p; }
DI s16x4 vtr(lds_cptr p) { return __builtin_amdgcn_ds_read_tr16_b64_v4i16((AS3 s16x4*)p); }
DI bf16x8 vread(lds_cptr p) {
  s16x4 a = vtr(p), b = vtr(p + 128);
  return __builtin_bit_cast(bf16x8, __builtin_shufflevector(a, b, 0, 1, 2, 3, 4, 5, 6, 7));
}
#define MFMA(a, b, c) __builtin_amdgcn_mfma_f32_32x32x16_bf16(a, b, c, 0, 0, 0)
DI bf16x8 qscale(u32x4 raw) {
  u32x4 w;
#pragma unroll
  for (int i = 0; i < 4; ++i) w[i] = cvtpk(__uint_as_float(raw[i] << 16) * QSCALE, __uint_as_float(raw[i] & 0xffff0000u) * QSCALE);
  return __builtin_bit_cast(bf16x8, w);
}
DI void store_rows(const f32x16& o, unsigned off, void* out) {
  const float rl = __builtin_amdgcn_rcpf(o[8]);
  unsigned w0 = cvtpk(o[0] * rl, o[1] * rl), w1 = cvtpk(o[2] * rl, o[3] * rl), w2 = cvtpk(o[4] * rl, o[5] * rl), w3 = cvtpk(o[6] * rl, o[7] * rl);
  auto r0 = __builtin_amdgcn_permlane32_swap(w0, w2, false, false);
  auto r1 = __builtin_amdgcn_permlane32_swap(w1, w3, false, false);
  u32x4 res = {r0[0], r1[0], r0[1], r1[1]};
  asm volatile("global_store_dwordx4 %0, %1, %2 sc1\n\ts_nop 1" :: "v"(off), "v"(res), "s"(out) : "memory");
}

#define S_(x) #x
#define STR(x) S_(x)
#define EX(A, i) "v_exp_f32 v[" STR(A) "+" STR(i) "], v[" STR(A) "+" STR(i) "]\n\t"
#define SMF(O, VA, P) "v_smfmac_f32_32x32x32_bf16 v[" STR(O) ":" STR(O) "+15], v[" STR(VA) ":" STR(VA) "+3], v[" STR(P) ":" STR(P) "+7], v116\n\t"
#define QK(B, Q, M) "v_mfma_f32_32x32x16_bf16 v[" STR(B) ":" STR(B) "+15], v[96:99], v[" STR(Q) ":" STR(Q) "+3], v[" STR(M) ":" STR(M) "+15]\n\t"
#define VRD(R, off) "ds_read_b64_tr_b16 v[" STR(R) ":" STR(R) "+1], v118 offset:" STR(off) "\n\tds_read_b64_tr_b16 v[" STR(R) "+2:" STR(R) "+3], v118 offset:" STR(off) "+128\n\t"
#define KRD(off) "ds_read_b128 v[96:99], v117 offset:" STR(off) "\n\t"
#define EX5(A, i) EX(A, (i)) EX(A, (i) + 1) EX(A, (i) + 2) EX(A, (i) + 3) EX(A, (i) + 4)
#define EX4(A, i) EX(A, (i)) EX(A, (i) + 1) EX(A, (i) + 2) EX(A, (i) + 3)
#define Y_HEAD EX(32, 0) EX(32, 1) SMF(0, 104, 48 + 8)
#define Y_REST(i) VRD(104, (i) * 1024 + 512) EX5(32, 2) QK(48, 108, 64) KRD(((i) + 1) * 1024) EX5(32, 7) "s_waitcnt lgkmcnt(3)\n\t" SMF(16, 100, 32) EX4(32, 12)
#define X_STEP_(i, EXTRA) EX(48, 0) EX(48, 1) "s_waitcnt lgkmcnt(0)\n\t" SMF(16, 104, 32 + 8) EX5(48, 2) QK(32, 112, 80) EX5(48, 7) SMF(0, 100, 48) VRD(100, ((i) + 1) * 1024) EXTRA EX4(48, 12)
#define X_STEP(i) X_STEP_(i, "")
#define SEND_OY \
  "s_cmp_lg_u32 %[j], 7\n\t" \
  "s_cbranch_scc1 6f\n\t" \
  "ds_write_b128 %[exw], v[16:19]\n\t" \
  "ds_write_b128 %[exw], v[20:23] offset:1024\n\t" \
  "ds_write_b32 %[exs], v24 offset:2048\n\t" \
  "ds_write_b32 %[fw], v116\n\t" \
  "6:\n\t"
#define MAIN_LOOP \
  "s_mov_b32 %[j], 0\n\t" \
  "s_setprio 3\n\t" \
  "s_nop 4\n\t" \
  EX(32, 0) EX(32, 1) \
  "s_branch 2f\n\t" \
  "1:\n\t" \
  Y_HEAD \
  "2:\n\t" \
  Y_REST(0) X_STEP(0) Y_HEAD Y_REST(1) X_STEP(1) Y_HEAD Y_REST(2) X_STEP(2) \
  "s_cmp_gt_i32 %[rdy], %[j]\n\t" \
  "s_cbranch_scc1 4f\n\t" \
  "3:\n\t" \
  "ds_read_b32 v121, v120\n\t" \
  "s_waitcnt lgkmcnt(0)\n\t" \
  "v_readfirstlane_b32 %[rdy], v121\n\t" \
  "s_nop 3\n\t" \
  "s_cmp_gt_i32 %[rdy], %[j]\n\t" \
  "s_cbranch_scc0 3b\n\t" \
  "4:\n\t" \
  "s_cmp_lg_u32 %[j], 7\n\t" \
  "s_cbranch_scc1 5f\n\t" \
  "s_setprio 0\n\t" \
  "5:\n\t" \
  Y_HEAD Y_REST(3) X_STEP_(3, SEND_OY) \
  "v_add_u32 v117, 0x1000, v117\n\t" \
  "v_add_u32 v118, v119, v118\n\t" \
  "s_add_u32 %[j], %[j], 1\n\t" \
  "s_cmp_lt_u32 %[j], 5\n\t" \
  "s_cbranch_scc1 1b\n\t" \
  "s_setprio 2\n\t" \
  "s_cmp_lt_u32 %[j], 7\n\t" \
  "s_cbranch_scc1 1b\n\t" \
  "s_setprio 1\n\t" \
  "s_cmp_lt_u32 %[j], 8\n\t" \
  "s_cbranch_scc1 1b\n\t" \
  "s_setprio 3\n\t" \
  SMF(0, 104, 48 + 8) \
  "s_branch 8f\n\t" \
  "7:\n\t" \
  "s_sleep 1\n\t" \
  "8:\n\t" \
  "ds_read_b32 v121, %[fr]\n\t" \
  "ds_read_b128 v[16:19], %[exr]\n\t" \
  "ds_read_b128 v[20:23], %[exr] offset:1024\n\t" \
  "ds_read_b32 v24, %[ext] offset:2048\n\t" \
  "s_waitcnt lgkmcnt(3)\n\t" \
  "v_cmp_eq_u32 vcc, v121, v116\n\t" \
  "s_nop 4\n\t" \
  "s_waitcnt lgkmcnt(0)\n\t" \
  "s_cbranch_vccz 7b\n\t"

__global__ __launch_bounds__(576, 2) void attn_fwd(const __hip_bfloat16* __restrict__ q, const __hip_bfloat16* __restrict__ k,
                                                   const __hip_bfloat16* __restrict__ v, __hip_bfloat16* __restrict__ out
                                                   ) {
  __shared__ __attribute__((aligned(1024))) char lds[LDS_BYTES];
  STAMP(0);
  const int tid = threadIdx.x, lane = tid & 63, r32 = lane & 31, hi = lane >> 5;
  const int wid = __builtin_amdgcn_readfirstlane(tid >> 6);
  const int b = blockIdx.x, head = (b & 7) * 4 + (b >> 6), qb = (b >> 3) & 7;
  const int qg = wid & 3, kh = (wid >> 2) & 1;
  const unsigned lds0 = (unsigned)(uintptr_t)lds;
  const unsigned hoff = head * (SEQ * DH * 2);
  const unsigned t0 = (kh * 32 + qg) * 1024;
  const unsigned frag = r32 * 32 + hi * 16;
  unsigned koff = hoff + t0 + frag, voff = hoff + t0 + lane * 16;
  unsigned qoff = hoff + qb * 8192 + (qg * 2 + kh) * 1024 + frag, qoff2 = qoff ^ 1024;
  unsigned mk = lds0 + LDS_K + t0, mv = lds0 + LDS_V + t0;
  asm volatile("" : "+v"(koff), "+v"(voff), "+v"(qoff2), "+s"(mk), "+s"(mv));
  SBAR();
  asm volatile("" : "+s"(q), "+s"(k), "+s"(v), "+s"(out));
  volatile AS3 int* flag = (volatile AS3 int*)((lds_ptr)lds + LDS_FLAG);
  if (wid == 8) {
    *flag = 0;
    const char* kb = (const char*)k; const char* vb = (const char*)v;
    __builtin_amdgcn_s_sleep(LSLEEP);
#pragma unroll
    for (int i = 0; i < 8; ++i) {
      if (i == LPRE) asm volatile("s_waitcnt lgkmcnt(0)\n\ts_barrier" ::: "memory");
      const int t = (4 + (i >> 1) + (i & 1) * 32) * 1024; glds16(kb + t, koff, mk + t); glds16(vb + t, voff, mv + t); }
    if (LPRE >= 8) asm volatile("s_waitcnt lgkmcnt(0)\n\ts_barrier" ::: "memory");
    for (int c = 2; c < 8; ++c) {
#pragma unroll
      for (int i = 0; i < 8; ++i) { const int t = (c * 4 + (i >> 1) + (i & 1) * 32) * 1024; glds16(kb + t, koff, mk + t); glds16(vb + t, voff, mv + t); }
      asm volatile("s_waitcnt vmcnt(16)" ::: "memory"); *flag = c - 1;
    }
    asm volatile("s_waitcnt vmcnt(0)" ::: "memory");
    *flag = 8;
    return;
  }
  u32x4 qraw2;
  glds16(k, koff, mk);
  asm volatile("global_load_dwordx4 %0, %1, %2" : "=v"(qraw2) : "v"(qoff2), "s"(q) : "memory");
  glds16(v, voff, mv);
  STAMP(1);
  if (tid < ONES_BYTES / 16) {
    const unsigned one = (tid & 1) ? 0u : 0x3f80u;
    u32x4 ones = {one, 0u, one, 0u};
    *(AS3 u32x4*)((lds_ptr)lds + LDS_ONES + tid * 16) = ones; }
  const bool ones_lane = (lane >> 4) & 1;
  const unsigned ka0 = lds0 + LDS_K + lane * 16;
  const unsigned va0 = lds0 + (ones_lane ? LDS_ONES + 128 + (lane & 2) * 8 : LDS_V + hi * 256 + (lane & 15) * 8);
  unsigned ka = ka0 + kh * 32768, vad = va0 + (ones_lane ? 0 : kh * 32768);
  int vstep = ones_lane ? 0 : 4096;
  int idx = 0xDDDD;
  unsigned fa = lds0 + LDS_FLAG;
  unsigned exw = lds0 + LDS_EX + wid * EX_SLOT + lane * 16, exs = lds0 + LDS_EX + wid * EX_SLOT + lane * 4;
  unsigned exr = lds0 + LDS_EX + (wid ^ 4) * EX_SLOT + lane * 16, ext = lds0 + LDS_EX + (wid ^ 4) * EX_SLOT + lane * 4;
  unsigned fw = lds0 + LDS_FLAG + 64 + wid * 4, fr = lds0 + LDS_FLAG + 64 + (wid ^ 4) * 4;
  *(AS3 unsigned*)(uintptr_t)fw = 0u;
  f32x16 ox = {}, oy = {};
  asm volatile("" : "+{v[0:15]}"(ox), "+{v[16:31]}"(oy), "+{v116}"(idx), "+{v119}"(vstep), "+{v120}"(fa), "+{v117}"(ka), "+{v118}"(vad),
                    "+v"(exw), "+v"(exs), "+v"(exr), "+v"(ext), "+v"(fw), "+v"(fr), "+v"(qoff));
  const f32x16 zero = {};
  unsigned ay = lds0 + LDS_K + t0 + hi * 512, ax = ay ^ 32768;
  asm volatile("s_waitcnt vmcnt(2)" : "+v"(ay), "+v"(ax) :: "memory");
  const bf16x8 kay = kread((lds_cptr)(uintptr_t)ay);
  asm volatile("s_waitcnt vmcnt(1)" : "+v"(qraw2) :: "memory");
  STAMP(2);
  const bf16x8 qy = qscale(qraw2);
  *(AS3 bf16x8*)(uintptr_t)exw = qy;
  f32x16 negy = MFMA(kay, __builtin_bit_cast(bf16x8, __builtin_bit_cast(u32x4, qy) ^ 0x80008000u), zero);
  SBAR();
  asm volatile("s_waitcnt vmcnt(0) lgkmcnt(0)\n\ts_barrier" ::: "memory");
  SBAR();
  STAMP(3);
  bf16x8 qx;
  { bf16x8 kf = kread((lds_cptr)(uintptr_t)ka); qx = kread((lds_cptr)(uintptr_t)exr);
    const bf16x8 kax = kread((lds_cptr)(uintptr_t)ax); bf16x8 va = vread((lds_cptr)(uintptr_t)vad);
    f32x16 sa = MFMA(kf, qy, negy);
    SBAR();
    f32x16 negx = MFMA(kax, __builtin_bit_cast(bf16x8, __builtin_bit_cast(u32x4, qx) ^ 0x80008000u), zero);
    int ready = 0, j;
    STAMP(4);
    asm volatile(MAIN_LOOP
      : "+{v[0:15]}"(ox), "+{v[16:31]}"(oy), "+{v[32:47]}"(sa), "+{v[96:99]}"(kf), "+{v[100:103]}"(va), "+{v117}"(ka), "+{v118}"(vad), [rdy] "+s"(ready), [j] "=&s"(j)
      : "{v[64:79]}"(negx), "{v[80:95]}"(negy), "{v[108:111]}"(qx), "{v[112:115]}"(qy), "{v116}"(idx), "{v119}"(vstep), "{v120}"(fa),
        [exw] "v"(exw), [exs] "v"(exs), [exr] "v"(exr), [ext] "v"(ext), [fw] "v"(fw), [fr] "v"(fr)
      : "v48", "v49", "v50", "v51", "v52", "v53", "v54", "v55", "v56", "v57", "v58", "v59", "v60", "v61", "v62", "v63", "v104", "v105", "v106", "v107", "v121", "vcc", "scc", "memory"); }
  STAMP(5);
  f32x16 o;
#pragma unroll
  for (int i = 0; i < 9; ++i) o[i] = ox[i] + oy[i];
  store_rows(o, qoff, out);
  SBAR();
  float chk = o[8];
#pragma unroll
  for (int i = 0; i < 8; ++i) chk = __builtin_fmaf(__builtin_fabsf(o[i]), 0x1p-40f, chk);
  if (__any(!(chk < 0x1p60f))) {
    const lds_cptr kp = (lds_cptr)(uintptr_t)ka0, vp = (lds_cptr)(uintptr_t)va0;
    float m = -INFINITY;
    for (int t = 0; t < NT; ++t) { const f32x16 s = MFMA(kread(kp + t * 1024), qx, zero);
#pragma unroll
      for (int i = 0; i < 16; ++i) m = __builtin_fmaxf(m, s[i]); }
    { auto rr = __builtin_amdgcn_permlane32_swap(__float_as_uint(m), __float_as_uint(m), false, false);
      m = __builtin_fmaxf(__uint_as_float(rr[0]), __uint_as_float(rr[1])); }
    o = zero;
    for (int t = 0; t < NT; ++t) { const f32x16 s = MFMA(kread(kp + t * 1024), qx, zero);
      const lds_cptr vt = vp + (ones_lane ? 0 : t * 1024);
#pragma unroll
      for (int h = 0; h < 2; ++h) { f32x8 p;
#pragma unroll
        for (int i = 0; i < 8; ++i) p[i] = __builtin_amdgcn_exp2f(s[8 * h + i] - m);
        o = __builtin_amdgcn_smfmac_f32_32x32x32_bf16(vread(vt + h * 512), __builtin_bit_cast(bf16x16, p), o, idx, 0, 0); } }
    store_rows(o, qoff, out);
  }
}

extern "C" void kernel_launch(void* const* d_in, const int* in_sizes, int n_in, void* d_out, int out_size, void* d_ws, size_t ws_size, hipStream_t stream) {
  attn_fwd<<<256, 576, 0, stream>>>((const __hip_bfloat16*)d_in[0], (const __hip_bfloat16*)d_in[1], (const __hip_bfloat16*)d_in[2], (__hip_bfloat16*)d_out
  );
}
```

```cpp
#include <hip/hip_runtime.h>
#include <hip/hip_bf16.h>
#include <stdint.h>

typedef __bf16 bf16x8 __attribute__((ext_vector_type(8)));
typedef __bf16 bf16x2 __attribute__((ext_vector_type(2)));
typedef short s16x4 __attribute__((ext_vector_type(4)));
typedef float f32x2 __attribute__((ext_vector_type(2)));
typedef float f32x16 __attribute__((ext_vector_type(16)));
typedef unsigned u32x4 __attribute__((ext_vector_type(4)));
#define AS3 __attribute__((address_space(3)))
typedef AS3 const char* lds_cptr;
typedef AS3 char* lds_ptr;

constexpr int SEQ = 2048, DH = 16;
constexpr int NT = SEQ / 32;
constexpr int LDS_K = 0, LDS_V = 65536, LDS_ONES = 131072, ONES_BYTES = 9216, LDS_FLAG = LDS_ONES + ONES_BYTES;
constexpr int LDS_BYTES = LDS_FLAG + 64;
constexpr float QSCALE = 0.25f * 1.4426950408889634f;

#define DI __device__ __forceinline__
#define SBAR() __builtin_amdgcn_sched_barrier(0)
#define PIN(x) asm volatile("" : "+v"(x))
#define STAMP(i) do { if ((i) == 0 || (i) == 4) { SBAR(); asm volatile("s_memrealtime %0\n\ts_waitcnt lgkmcnt(0)" : "=s"(st[i]) :: "memory"); SBAR(); } } while (0)

DI void glds16(const void* sbase, unsigned voff, unsigned lds_base) {
  asm volatile("s_mov_b32 m0, %2\n\ts_nop 0\n\tglobal_load_lds_dwordx4 %0, %1" :: "v"(voff), "s"(sbase), "s"(lds_base) : "memory");
}
DI unsigned cvtpk(float lo, float hi) { f32x2 v = {lo, hi}; bf16x2 b = __builtin_convertvector(v, bf16x2); return __builtin_bit_cast(unsigned, b); }
DI bf16x8 kread(lds_cptr p) { return *(const AS3 bf16x8*)p; }
DI s16x4 vtr(lds_cptr p) { return __builtin_amdgcn_ds_read_tr16_b64_v4i16((AS3 s16x4*)p); }
DI bf16x8 vread(lds_cptr p) {
  s16x4 a = vtr(p), b = vtr(p + 128);
  return __builtin_bit_cast(bf16x8, __builtin_shufflevector(a, b, 0, 1, 2, 3, 4, 5, 6, 7));
}
#define MFMA(a, b, c) __builtin_amdgcn_mfma_f32_32x32x16_bf16(a, b, c, 0, 0, 0)
DI float max3(float a, float b, float c) { return __builtin_fmaxf(__builtin_fmaxf(a, b), c); }
DI float max16(const f32x16& s) {
  const float a = max3(s[0], s[1], s[2]), b = max3(s[3], s[4], s[5]), c = max3(s[6], s[7], s[8]), d = max3(s[9], s[10], s[11]), e = max3(s[12], s[13], s[14]);
  return __builtin_fmaxf(max3(a, b, c), max3(d, e, s[15]));
}
DI float xhalf_max(float m) {
  auto rr = __builtin_amdgcn_permlane32_swap(__float_as_uint(m), __float_as_uint(m), false, false);
  return __builtin_fmaxf(__uint_as_float(rr[0]), __uint_as_float(rr[1]));
}

#define S_(x) #x
#define STR(x) S_(x)
#define EX(A, i) "v_exp_f32 v[" STR(A) "+" STR(i) "], v[" STR(A) "+" STR(i) "]\n\t"
#define SMF(VA, P) "v_smfmac_f32_32x32x32_bf16 v[0:15], v[" STR(VA) ":" STR(VA) "+3], v[" STR(P) ":" STR(P) "+7], v80\n\t"
#define QK(B) "v_mfma_f32_32x32x16_bf16 v[" STR(B) ":" STR(B) "+15], v[64:67], v[76:79], v[16:31]\n\t"
#define VRD(R, off) "ds_read_b64_tr_b16 v[" STR(R) ":" STR(R) "+1], v82 offset:" STR(off) "\n\tds_read_b64_tr_b16 v[" STR(R) "+2:" STR(R) "+3], v82 offset:" STR(off) "+128\n\t"
#define KRD(off) "ds_read_b128 v[64:67], v81 offset:" STR(off) "\n\t"
#define TILE_HEAD(A, B) EX(A, 0) EX(A, 1) "s_waitcnt lgkmcnt(0)\n\t" SMF(72, B + 8)
#define TILE_REST(A, B, i) VRD(72, (i) * 1024 + 512) EX(A, 2) EX(A, 3) EX(A, 4) EX(A, 5) EX(A, 6) QK(B) KRD(((i) + 2) * 1024) \
  EX(A, 7) EX(A, 8) EX(A, 9) EX(A, 10) EX(A, 11) SMF(68, A) VRD(68, ((i) + 1) * 1024) EX(A, 12) EX(A, 13) EX(A, 14) EX(A, 15)
#define TILE_E(i) TILE_HEAD(32, 48) TILE_REST(32, 48, i)
#define TILE_O(i) TILE_HEAD(48, 32) TILE_REST(48, 32, i)
#define MAIN_LOOP \
  "s_mov_b32 %[j], 0\n\t" \
  "s_setprio 3\n\t" \
  "s_nop 9\n\t" \
  EX(32, 0) EX(32, 1) \
  "s_branch 2f\n\t" \
  "1:\n\t" \
  TILE_HEAD(32, 48) \
  "2:\n\t" \
  TILE_REST(32, 48, 0) TILE_O(1) TILE_E(2) TILE_O(3) TILE_E(4) TILE_O(5) \
  "s_cmp_gt_i32 %[rdy], %[j]\n\t" \
  "s_cbranch_scc1 4f\n\t" \
  "3:\n\t" \
  "ds_read_b32 v85, v84\n\t" \
  "s_waitcnt lgkmcnt(0)\n\t" \
  "v_readfirstlane_b32 %[rdy], v85\n\t" \
  "s_nop 3\n\t" \
  "s_cmp_gt_i32 %[rdy], %[j]\n\t" \
  "s_cbranch_scc0 3b\n\t" \
  "4:\n\t" \
  "s_cmp_lg_u32 %[j], 7\n\t" \
  "s_cbranch_scc1 5f\n\t" \
  "s_setprio 0\n\t" \
  "5:\n\t" \
  TILE_E(6) TILE_O(7) \
  "v_add_u32 v81, 0x2000, v81\n\t" \
  "v_add_u32 v82, v83, v82\n\t" \
  "s_add_u32 %[j], %[j], 1\n\t" \
  "s_cmp_lt_u32 %[j], 5\n\t" \
  "s_cbranch_scc1 1b\n\t" \
  "s_setprio 2\n\t" \
  "s_cmp_lt_u32 %[j], 7\n\t" \
  "s_cbranch_scc1 1b\n\t" \
  "s_setprio 1\n\t" \
  "s_cmp_lt_u32 %[j], 8\n\t" \
  "s_cbranch_scc1 1b\n\t" \
  "s_setprio 3\n\t" \
  "s_waitcnt lgkmcnt(0)\n\t" \
  SMF(72, 48 + 8) \
  "s_nop 11\n\t"

__global__ __launch_bounds__(576, 2) void attn_fwd(const __hip_bfloat16* __restrict__ q, const __hip_bfloat16* __restrict__ k,
                                                   const __hip_bfloat16* __restrict__ v, __hip_bfloat16* __restrict__ out
                                                   ) {
  __shared__ __attribute__((aligned(1024))) char lds[LDS_BYTES];
  unsigned long long st[8];
  STAMP(0);
  const int tid = threadIdx.x, lane = tid & 63, r32 = lane & 31, hi = lane >> 5;
  const int wid = __builtin_amdgcn_readfirstlane(tid >> 6);
  const int b = blockIdx.x, head = (b & 7) * 4 + (b >> 6), qb = (b >> 3) & 7;
  const unsigned lds0 = (unsigned)(uintptr_t)lds;
  const unsigned hoff = head * (SEQ * DH * 2);
  unsigned koff = hoff + wid * 1024 + r32 * 32 + hi * 16, voff = hoff + wid * 1024 + lane * 16;
  unsigned qoff = koff + qb * 8192, aoff = hoff + hi * 16;
  unsigned mk = lds0 + LDS_K + wid * 1024, mv = lds0 + LDS_V + wid * 1024;
  asm volatile("" : "+v"(koff), "+v"(voff), "+v"(qoff), "+v"(aoff), "+s"(mk), "+s"(mv));
  SBAR();
  asm volatile("" : "+s"(q), "+s"(k), "+s"(v), "+s"(out));
  volatile AS3 int* flag = (volatile AS3 int*)((lds_ptr)lds + LDS_FLAG);
  if (wid == 8) {
    *flag = 0;
    const char* kb = (const char*)k; const char* vb = (const char*)v;
    __builtin_amdgcn_s_sleep(2);
#pragma unroll
    for (int i = 0; i < 8; ++i) { glds16(kb + i * 1024, koff, mk + i * 1024); glds16(vb + i * 1024, voff, mv + i * 1024); }
    asm volatile("s_waitcnt lgkmcnt(0)\n\ts_barrier" ::: "memory");
    for (int c = 1; c < 7; ++c) {
#pragma unroll
      for (int i = 0; i < 8; ++i) { glds16(kb + (c * 8 + i) * 1024, koff, mk + (c * 8 + i) * 1024); glds16(vb + (c * 8 + i) * 1024, voff, mv + (c * 8 + i) * 1024); }
      asm volatile("s_waitcnt vmcnt(16)" ::: "memory");
      *flag = c;
    }
    asm volatile("s_waitcnt vmcnt(0)" ::: "memory");
    *flag = 8;
    return;
  }
  u32x4 qraw, araw;
  asm volatile("global_load_dwordx4 %0, %2, %4\n\tglobal_load_dwordx4 %1, %3, %5" : "=&v"(qraw), "=&v"(araw) : "v"(qoff), "v"(aoff), "s"(q), "s"(k) : "memory");
  glds16(k, koff, mk);
  glds16(v, voff, mv);
  STAMP(1);
  { const unsigned one = (tid & 1) ? 0u : 0x3f80u;
    u32x4 ones = {one, 0u, one, 0u};
    *(AS3 u32x4*)((lds_ptr)lds + LDS_ONES + tid * 16) = ones;
    if (tid < 64) *(AS3 u32x4*)((lds_ptr)lds + LDS_ONES + 8192 + tid * 16) = ones; }
  unsigned ka0 = lds0 + LDS_K + lane * 16;
  const bool ones_lane = (lane >> 4) & 1;
  unsigned va0 = lds0 + (ones_lane ? LDS_ONES + 128 : LDS_V + hi * 256) + (lane & 15) * 8;
  int vstep = ones_lane ? 0 : 8192;
  int idx = 0xDDDD;
  unsigned fa = lds0 + LDS_FLAG;
  f32x16 o = {};
  asm volatile("" : "+{v[0:15]}"(o), "+{v80}"(idx), "+{v83}"(vstep), "+{v84}"(fa), "+v"(ka0), "+v"(va0));
  const f32x16 zero = {};
  asm volatile("s_waitcnt vmcnt(2)" : "+v"(qraw), "+v"(araw) :: "memory");
  STAMP(2);
  bf16x8 qf;
  { u32x4 w;
#pragma unroll
    for (int i = 0; i < 4; ++i) w[i] = cvtpk(__uint_as_float(qraw[i] << 16) * QSCALE, __uint_as_float(qraw[i] & 0xffff0000u) * QSCALE);
    qf = __builtin_bit_cast(bf16x8, w); }
  const lds_cptr kp = (lds_cptr)(uintptr_t)ka0, vp = (lds_cptr)(uintptr_t)va0;
  f32x16 negm = MFMA(__builtin_bit_cast(bf16x8, araw ^ 0x80008000u), qf, zero);
  asm volatile("s_waitcnt vmcnt(0) lgkmcnt(0)\n\ts_barrier" ::: "memory");
  STAMP(3);
  int ready = 0;
  for (int attempt = 0;; ++attempt) {
    bf16x8 k0 = kread(kp), kf = kread(kp + 1024), va = vread(vp);
    f32x16 sa = MFMA(k0, qf, negm);
    unsigned ka = ka0, vad = va0; int j;
    STAMP(4);
    asm volatile(MAIN_LOOP
      : "+{v[0:15]}"(o), "+{v[32:47]}"(sa), "+{v[64:67]}"(kf), "+{v[68:71]}"(va), "+{v81}"(ka), "+{v82}"(vad), [rdy] "+s"(ready), [j] "=&s"(j)
      : "{v[16:31]}"(negm), "{v[76:79]}"(qf), "{v80}"(idx), "{v83}"(vstep), "{v84}"(fa)
      : "v48", "v49", "v50", "v51", "v52", "v53", "v54", "v55", "v56", "v57", "v58", "v59", "v60", "v61", "v62", "v63", "v72", "v73", "v74", "v75", "v85", "scc", "memory");
    STAMP(5);
    const float rl = __builtin_amdgcn_rcpf(o[8]);
    unsigned w0 = cvtpk(o[0] * rl, o[1] * rl), w1 = cvtpk(o[2] * rl, o[3] * rl), w2 = cvtpk(o[4] * rl, o[5] * rl), w3 = cvtpk(o[6] * rl, o[7] * rl);
    auto r0 = __builtin_amdgcn_permlane32_swap(w0, w2, false, false);
    auto r1 = __builtin_amdgcn_permlane32_swap(w1, w3, false, false);
    u32x4 res = {r0[0], r1[0], r0[1], r1[1]};
    asm volatile("global_store_dwordx4 %0, %1, %2 sc1\n\ts_nop 1" :: "v"(qoff), "v"(res), "s"(out) : "memory");
    SBAR();
    float chk = o[8];
#pragma unroll
    for (int i = 0; i < 8; ++i) chk = __builtin_fmaf(__builtin_fabsf(o[i]), 0x1p-40f, chk);
    if (attempt || !__any(!(chk < 0x1p60f))) break;
    float mm = -INFINITY;
    for (int t = 0; t < NT; ++t) mm = __builtin_fmaxf(mm, max16(MFMA(kread(kp + t * 1024), qf, zero)));
    const float m = xhalf_max(mm);
#pragma unroll
    for (int i = 0; i < 16; ++i) negm[i] = -m;
    o = zero;
  }
  { unsigned long long tend = __builtin_amdgcn_s_memrealtime() + 0 * (st[4] - st[0]); while (__builtin_amdgcn_s_memrealtime() < tend) __builtin_amdgcn_s_sleep(1); }
}

extern "C" void kernel_launch(void* const* d_in, const int* in_sizes, int n_in, void* d_out, int out_size, void* d_ws, size_t ws_size, hipStream_t stream) {
  attn_fwd<<<256, 576, 0, stream>>>((const __hip_bfloat16*)d_in[0], (const __hip_bfloat16*)d_in[1], (const __hip_bfloat16*)d_in[2], (__hip_bfloat16*)d_out
  );
}
```

```cpp
#include <hip/hip_runtime.h>
#include <hip/hip_bf16.h>
#include <stdint.h>

typedef __bf16 bf16x8 __attribute__((ext_vector_type(8)));
typedef __bf16 bf16x16 __attribute__((ext_vector_type(16)));
typedef __bf16 bf16x2 __attribute__((ext_vector_type(2)));
typedef short s16x4 __attribute__((ext_vector_type(4)));
typedef float f32x2 __attribute__((ext_vector_type(2)));
typedef float f32x4 __attribute__((ext_vector_type(4)));
typedef float f32x8 __attribute__((ext_vector_type(8)));
typedef float f32x16 __attribute__((ext_vector_type(16)));
typedef unsigned u32x4 __attribute__((ext_vector_type(4)));
typedef unsigned u32x8 __attribute__((ext_vector_type(8)));
#define AS3 __attribute__((address_space(3)))
typedef AS3 const char* lds_cptr;
typedef AS3 char* lds_ptr;

#define LSLEEP 2
#define LPRE 2
constexpr int SEQ = 2048, DH = 16;
constexpr int NT = SEQ / 32;
constexpr int LDS_K = 0, LDS_V = 65536, LDS_ONES = 131072, ONES_BYTES = 5120, LDS_FLAG = LDS_ONES + ONES_BYTES;
constexpr int LDS_EX = LDS_FLAG + 256, EX_SLOT = 2304, LDS_BYTES = LDS_EX + 8 * EX_SLOT;
constexpr float QSCALE = 0.25f * 1.4426950408889634f;

#define DI __device__ __forceinline__
#define SBAR() __builtin_amdgcn_sched_barrier(0)
#define STAMP(i)

DI void glds16(const void* sbase, unsigned voff, unsigned lds_base) {
  asm volatile("s_mov_b32 m0, %2\n\ts_nop 0\n\tglobal_load_lds_dwordx4 %0, %1" :: "v"(voff), "s"(sbase), "s"(lds_base) : "memory");
}
DI unsigned cvtpk(float lo, float hi) { f32x2 v = {lo, hi}; bf16x2 b = __builtin_convertvector(v, bf16x2); return __builtin_bit_cast(unsigned, b); }
DI bf16x8 kread(lds_cptr p) { return *(const AS3 bf16x8*)p; }
DI s16x4 vtr(lds_cptr p) { return __builtin_amdgcn_ds_read_tr16_b64_v4i16((AS3 s16x4*)p); }
DI bf16x8 vread(lds_cptr p) {
  s16x4 a = vtr(p), b = vtr(p + 128);
  return __builtin_bit_cast(bf16x8, __builtin_shufflevector(a, b, 0, 1, 2, 3, 4, 5, 6, 7));
}
#define MFMA(a, b, c) __builtin_amdgcn_mfma_f32_32x32x16_bf16(a, b, c, 0, 0, 0)
DI bf16x8 qscale(u32x4 raw) {
  u32x4 w;
#pragma unroll
  for (int i = 0; i < 4; ++i) w[i] = cvtpk(__uint_as_float(raw[i] << 16) * QSCALE, __uint_as_float(raw[i] & 0xffff0000u) * QSCALE);
  return __builtin_bit_cast(bf16x8, w);
}
DI void store_rows(const f32x16& o, unsigned off, void* out) {
  const float rl = __builtin_amdgcn_rcpf(o[8]);
  unsigned w0 = cvtpk(o[0] * rl, o[1] * rl), w1 = cvtpk(o[2] * rl, o[3] * rl), w2 = cvtpk(o[4] * rl, o[5] * rl), w3 = cvtpk(o[6] * rl, o[7] * rl);
  auto r0 = __builtin_amdgcn_permlane32_swap(w0, w2, false, false);
  auto r1 = __builtin_amdgcn_permlane32_swap(w1, w3, false, false);
  u32x4 res = {r0[0], r1[0], r0[1], r1[1]};
  asm volatile("global_store_dwordx4 %0, %1, %2 sc1\n\ts_nop 1" :: "v"(off), "v"(res), "s"(out) : "memory");
}

#define S_(x) #x
#define STR(x) S_(x)
#define EX(A, i) "v_exp_f32 v[" STR(A) "+" STR(i) "], v[" STR(A) "+" STR(i) "]\n\t"
#define SMF(O, VA, P) "v_smfmac_f32_32x32x32_bf16 v[" STR(O) ":" STR(O) "+15], v[" STR(VA) ":" STR(VA) "+3], v[" STR(P) ":" STR(P) "+7], v116\n\t"
#define QK(B, Q, M) "v_mfma_f32_32x32x16_bf16 v[" STR(B) ":" STR(B) "+15], v[96:99], v[" STR(Q) ":" STR(Q) "+3], v[" STR(M) ":" STR(M) "+15]\n\t"
#define VRD(R, off) "ds_read_b64_tr_b16 v[" STR(R) ":" STR(R) "+1], v118 offset:" STR(off) "\n\tds_read_b64_tr_b16 v[" STR(R) "+2:" STR(R) "+3], v118 offset:" STR(off) "+128\n\t"
#define KRD(off) "ds_read_b128 v[96:99], v117 offset:" STR(off) "\n\t"
#define EX5(A, i) EX(A, (i)) EX(A, (i) + 1) EX(A, (i) + 2) EX(A, (i) + 3) EX(A, (i) + 4)
#define EX4(A, i) EX(A, (i)) EX(A, (i) + 1) EX(A, (i) + 2) EX(A, (i) + 3)
#define Y_HEAD EX(32, 0) EX(32, 1) SMF(0, 104, 48 + 8)
#define Y_REST(i) VRD(104, (i) * 1024 + 512) EX5(32, 2) QK(48, 108, 64) KRD(((i) + 1) * 1024) EX5(32, 7) "s_waitcnt lgkmcnt(3)\n\t" SMF(16, 100, 32) EX4(32, 12)
#define X_STEP_(i, EXTRA) EX(48, 0) EX(48, 1) "s_waitcnt lgkmcnt(0)\n\t" SMF(16, 104, 32 + 8) EX5(48, 2) QK(32, 112, 80) EX5(48, 7) SMF(0, 100, 48) VRD(100, ((i) + 1) * 1024) EXTRA EX4(48, 12)
#define X_STEP(i) X_STEP_(i, "")
#define SEND_OY \
  "s_cmp_lg_u32 %[j], 7\n\t" \
  "s_cbranch_scc1 6f\n\t" \
  "ds_write_b128 %[exw], v[16:19]\n\t" \
  "ds_write_b128 %[exw], v[20:23] offset:1024\n\t" \
  "ds_write_b32 %[exs], v24 offset:2048\n\t" \
  "ds_write_b32 %[fw], v116\n\t" \
  "6:\n\t"
#define MAIN_LOOP \
  "s_mov_b32 %[j], 0\n\t" \
  "s_setprio 3\n\t" \
  "s_nop 4\n\t" \
  EX(32, 0) EX(32, 1) \
  "s_branch 2f\n\t" \
  "1:\n\t" \
  Y_HEAD \
  "2:\n\t" \
  Y_REST(0) X_STEP(0) Y_HEAD Y_REST(1) X_STEP(1) Y_HEAD Y_REST(2) X_STEP(2) \
  "s_cmp_gt_i32 %[rdy], %[j]\n\t" \
  "s_cbranch_scc1 4f\n\t" \
  "3:\n\t" \
  "ds_read_b32 v121, v120\n\t" \
  "s_waitcnt lgkmcnt(0)\n\t" \
  "v_readfirstlane_b32 %[rdy], v121\n\t" \
  "s_nop 3\n\t" \
  "s_cmp_gt_i32 %[rdy], %[j]\n\t" \
  "s_cbranch_scc0 3b\n\t" \
  "4:\n\t" \
  "s_cmp_lg_u32 %[j], 7\n\t" \
  "s_cbranch_scc1 5f\n\t" \
  "s_setprio 0\n\t" \
  "5:\n\t" \
  Y_HEAD Y_REST(3) X_STEP_(3, SEND_OY) \
  "v_add_u32 v117, 0x1000, v117\n\t" \
  "v_add_u32 v118, v119, v118\n\t" \
  "s_add_u32 %[j], %[j], 1\n\t" \
  "s_cmp_lt_u32 %[j], 5\n\t" \
  "s_cbranch_scc1 1b\n\t" \
  "s_setprio 2\n\t" \
  "s_cmp_lt_u32 %[j], 7\n\t" \
  "s_cbranch_scc1 1b\n\t" \
  "s_setprio 1\n\t" \
  "s_cmp_lt_u32 %[j], 8\n\t" \
  "s_cbranch_scc1 1b\n\t" \
  "s_setprio 3\n\t" \
  SMF(0, 104, 48 + 8) \
  "s_branch 8f\n\t" \
  "7:\n\t" \
  "s_sleep 1\n\t" \
  "8:\n\t" \
  "ds_read_b32 v121, %[fr]\n\t" \
  "ds_read_b128 v[16:19], %[exr]\n\t" \
  "ds_read_b128 v[20:23], %[exr] offset:1024\n\t" \
  "ds_read_b32 v24, %[ext] offset:2048\n\t" \
  "s_waitcnt lgkmcnt(3)\n\t" \
  "v_cmp_eq_u32 vcc, v121, v116\n\t" \
  "s_nop 4\n\t" \
  "s_waitcnt lgkmcnt(0)\n\t" \
  "s_cbranch_vccz 7b\n\t"

__global__ __launch_bounds__(576, 2) void attn_fwd(const __hip_bfloat16* __restrict__ q, const __hip_bfloat16* __restrict__ k,
                                                   const __hip_bfloat16* __restrict__ v, __hip_bfloat16* __restrict__ out
                                                   ) {
  __shared__ __attribute__((aligned(1024))) char lds[LDS_BYTES];
  STAMP(0);
  const int tid = threadIdx.x, lane = tid & 63, r32 = lane & 31, hi = lane >> 5;
  const int wid = __builtin_amdgcn_readfirstlane(tid >> 6);
  unsigned pf;
  { unsigned po = lane * 128; po = po < 4480 ? po : 0; unsigned long long pc;
    asm volatile("s_cmp_lg_u32 %[w], 8\n\ts_cbranch_scc1 1f\n\ts_getpc_b64 %[pc]\n\tglobal_load_dword v85, %[o], %[pc]\n\t1:" : "={v85}"(pf), [pc] "=&s"(pc) : [o] "v"(po), [w] "s"(wid) : "scc", "memory"); }
  const int b = blockIdx.x, head = (b & 7) * 4 + (b >> 6), qb = (b >> 3) & 7;
  const int qg = wid & 3, kh = (wid >> 2) & 1;
  const unsigned lds0 = (unsigned)(uintptr_t)lds;
  const unsigned hoff = head * (SEQ * DH * 2);
  const unsigned t0 = (kh * 32 + qg) * 1024;
  const unsigned frag = r32 * 32 + hi * 16;
  unsigned koff = hoff + t0 + frag, voff = hoff + t0 + lane * 16;
  unsigned qoff = hoff + qb * 8192 + (qg * 2 + kh) * 1024 + frag, qoff2 = qoff ^ 1024;
  unsigned mk = lds0 + LDS_K + t0, mv = lds0 + LDS_V + t0;
  asm volatile("" : "+v"(koff), "+v"(voff), "+v"(qoff2), "+s"(mk), "+s"(mv));
  SBAR();
  asm volatile("" : "+s"(q), "+s"(k), "+s"(v), "+s"(out));
  volatile AS3 int* flag = (volatile AS3 int*)((lds_ptr)lds + LDS_FLAG);
  if (wid == 8) {
    *flag = 0;
    const char* kb = (const char*)k; const char* vb = (const char*)v;
    __builtin_amdgcn_s_sleep(LSLEEP);
#pragma unroll
    for (int i = 0; i < 8; ++i) {
      if (i == LPRE) asm volatile("s_waitcnt lgkmcnt(0)\n\ts_barrier" ::: "memory");
      const int t = (4 + (i >> 1) + (i & 1) * 32) * 1024; glds16(kb + t, koff, mk + t); glds16(vb + t, voff, mv + t); }
    if (LPRE >= 8) asm volatile("s_waitcnt lgkmcnt(0)\n\ts_barrier" ::: "memory");
    for (int c = 2; c < 8; ++c) {
#pragma unroll
      for (int i = 0; i < 8; ++i) { const int t = (c * 4 + (i >> 1) + (i & 1) * 32) * 1024; glds16(kb + t, koff, mk + t); glds16(vb + t, voff, mv + t); }
      asm volatile("s_waitcnt vmcnt(16)" ::: "memory"); *flag = c - 1;
    }
    asm volatile("s_waitcnt vmcnt(0)" : "+{v85}"(pf) :: "memory");
    *flag = 8;
    return;
  }
  u32x4 qraw2;
  glds16(k, koff, mk);
  asm volatile("global_load_dwordx4 %0, %1, %2" : "=v"(qraw2) : "v"(qoff2), "s"(q) : "memory");
  glds16(v, voff, mv);
  STAMP(1);
  if (tid < ONES_BYTES / 16) {
    const unsigned one = (tid & 1) ? 0u : 0x3f80u;
    u32x4 ones = {one, 0u, one, 0u};
    *(AS3 u32x4*)((lds_ptr)lds + LDS_ONES + tid * 16) = ones; }
  const bool ones_lane = (lane >> 4) & 1;
  const unsigned ka0 = lds0 + LDS_K + lane * 16;
  const unsigned va0 = lds0 + (ones_lane ? LDS_ONES + 128 + (lane & 2) * 8 : LDS_V + hi * 256 + (lane & 15) * 8);
  unsigned ka = ka0 + kh * 32768, vad = va0 + (ones_lane ? 0 : kh * 32768);
  int vstep = ones_lane ? 0 : 4096;
  int idx = 0xDDDD;
  unsigned fa = lds0 + LDS_FLAG;
  unsigned exw = lds0 + LDS_EX + wid * EX_SLOT + lane * 16, exs = lds0 + LDS_EX + wid * EX_SLOT + lane * 4;
  unsigned exr = lds0 + LDS_EX + (wid ^ 4) * EX_SLOT + lane * 16, ext = lds0 + LDS_EX + (wid ^ 4) * EX_SLOT + lane * 4;
  unsigned fw = lds0 + LDS_FLAG + 64 + wid * 4, fr = lds0 + LDS_FLAG + 64 + (wid ^ 4) * 4;
  *(AS3 unsigned*)(uintptr_t)fw = 0u;
  f32x16 ox = {}, oy = {};
  asm volatile("" : "+{v[0:15]}"(ox), "+{v[16:31]}"(oy), "+{v116}"(idx), "+{v119}"(vstep), "+{v120}"(fa), "+{v117}"(ka), "+{v118}"(vad),
                    "+v"(exw), "+v"(exs), "+v"(exr), "+v"(ext), "+v"(fw), "+v"(fr), "+v"(qoff));
  const f32x16 zero = {};
  unsigned ay = lds0 + LDS_K + t0 + hi * 512, ax = ay ^ 32768;
  asm volatile("s_waitcnt vmcnt(2)" : "+v"(ay), "+v"(ax) :: "memory");
  const bf16x8 kay = kread((lds_cptr)(uintptr_t)ay);
  asm volatile("s_waitcnt vmcnt(1)" : "+v"(qraw2) :: "memory");
  STAMP(2);
  const bf16x8 qy = qscale(qraw2);
  *(AS3 bf16x8*)(uintptr_t)exw = qy;
  f32x16 negy = MFMA(kay, __builtin_bit_cast(bf16x8, __builtin_bit_cast(u32x4, qy) ^ 0x80008000u), zero);
  SBAR();
  asm volatile("s_waitcnt vmcnt(0) lgkmcnt(0)\n\ts_barrier" ::: "memory");
  SBAR();
  STAMP(3);
  bf16x8 qx;
  { bf16x8 kf = kread((lds_cptr)(uintptr_t)ka); qx = kread((lds_cptr)(uintptr_t)exr);
    const bf16x8 kax = kread((lds_cptr)(uintptr_t)ax); bf16x8 va = vread((lds_cptr)(uintptr_t)vad);
    f32x16 sa = MFMA(kf, qy, negy);
    SBAR();
    f32x16 negx = MFMA(kax, __builtin_bit_cast(bf16x8, __builtin_bit_cast(u32x4, qx) ^ 0x80008000u), zero);
    int ready = 0, j;
    STAMP(4);
    asm volatile(MAIN_LOOP
      : "+{v[0:15]}"(ox), "+{v[16:31]}"(oy), "+{v[32:47]}"(sa), "+{v[96:99]}"(kf), "+{v[100:103]}"(va), "+{v117}"(ka), "+{v118}"(vad), [rdy] "+s"(ready), [j] "=&s"(j)
      : "{v[64:79]}"(negx), "{v[80:95]}"(negy), "{v[108:111]}"(qx), "{v[112:115]}"(qy), "{v116}"(idx), "{v119}"(vstep), "{v120}"(fa),
        [exw] "v"(exw), [exs] "v"(exs), [exr] "v"(exr), [ext] "v"(ext), [fw] "v"(fw), [fr] "v"(fr)
      : "v48", "v49", "v50", "v51", "v52", "v53", "v54", "v55", "v56", "v57", "v58", "v59", "v60", "v61", "v62", "v63", "v104", "v105", "v106", "v107", "v121", "vcc", "scc", "memory"); }
  STAMP(5);
  f32x16 o;
#pragma unroll
  for (int i = 0; i < 9; ++i) o[i] = ox[i] + oy[i];
  store_rows(o, qoff, out);
  SBAR();
  float chk = o[8];
#pragma unroll
  for (int i = 0; i < 8; ++i) chk = __builtin_fmaf(__builtin_fabsf(o[i]), 0x1p-40f, chk);
  if (__any(!(chk < 0x1p60f))) {
    const lds_cptr kp = (lds_cptr)(uintptr_t)ka0, vp = (lds_cptr)(uintptr_t)va0;
    float m = -INFINITY;
    for (int t = 0; t < NT; ++t) { const f32x16 s = MFMA(kread(kp + t * 1024), qx, zero);
#pragma unroll
      for (int i = 0; i < 16; ++i) m = __builtin_fmaxf(m, s[i]); }
    { auto rr = __builtin_amdgcn_permlane32_swap(__float_as_uint(m), __float_as_uint(m), false, false);
      m = __builtin_fmaxf(__uint_as_float(rr[0]), __uint_as_float(rr[1])); }
    o = zero;
    for (int t = 0; t < NT; ++t) { const f32x16 s = MFMA(kread(kp + t * 1024), qx, zero);
      const lds_cptr vt = vp + (ones_lane ? 0 : t * 1024);
#pragma unroll
      for (int h = 0; h < 2; ++h) { f32x8 p;
#pragma unroll
        for (int i = 0; i < 8; ++i) p[i] = __builtin_amdgcn_exp2f(s[8 * h + i] - m);
        o = __builtin_amdgcn_smfmac_f32_32x32x32_bf16(vread(vt + h * 512), __builtin_bit_cast(bf16x16, p), o, idx, 0, 0); } }
    store_rows(o, qoff, out);
  }
}

extern "C" void kernel_launch(void* const* d_in, const int* in_sizes, int n_in, void* d_out, int out_size, void* d_ws, size_t ws_size, hipStream_t stream) {
  attn_fwd<<<256, 576, 0, stream>>>((const __hip_bfloat16*)d_in[0], (const __hip_bfloat16*)d_in[1], (const __hip_bfloat16*)d_in[2], (__hip_bfloat16*)d_out
  );
}
```

```cpp
#include <hip/hip_runtime.h>
#include <hip/hip_bf16.h>
#include <stdint.h>

typedef __bf16 bf16x8 __attribute__((ext_vector_type(8)));
typedef __bf16 bf16x2 __attribute__((ext_vector_type(2)));
typedef short s16x4 __attribute__((ext_vector_type(4)));
typedef float f32x2 __attribute__((ext_vector_type(2)));
typedef float f32x16 __attribute__((ext_vector_type(16)));
typedef unsigned u32x4 __attribute__((ext_vector_type(4)));
#define AS3 __attribute__((address_space(3)))
typedef AS3 const char* lds_cptr;
typedef AS3 char* lds_ptr;

constexpr int SEQ = 2048, DH = 16;
constexpr int NT = SEQ / 32;
constexpr int LDS_K = 0, LDS_V = 65536, LDS_ONES = 131072, ONES_BYTES = 9216, LDS_FLAG = LDS_ONES + ONES_BYTES;
constexpr int LDS_BYTES = LDS_FLAG + 64;
constexpr float QSCALE = 0.25f * 1.4426950408889634f;

#define DI __device__ __forceinline__
#define SBAR() __builtin_amdgcn_sched_barrier(0)
#define PIN(x) asm volatile("" : "+v"(x))
#define STAMP(i) do { if ((i) == 0 || (i) == 1) { SBAR(); asm volatile("s_memrealtime %0\n\ts_waitcnt lgkmcnt(0)" : "=s"(st[i]) :: "memory"); SBAR(); } } while (0)

DI void glds16(const void* sbase, unsigned voff, unsigned lds_base) {
  asm volatile("s_mov_b32 m0, %2\n\ts_nop 0\n\tglobal_load_lds_dwordx4 %0, %1" :: "v"(voff), "s"(sbase), "s"(lds_base) : "memory");
}
DI unsigned cvtpk(float lo, float hi) { f32x2 v = {lo, hi}; bf16x2 b = __builtin_convertvector(v, bf16x2); return __builtin_bit_cast(unsigned, b); }
DI bf16x8 kread(lds_cptr p) { return *(const AS3 bf16x8*)p; }
DI s16x4 vtr(lds_cptr p) { return __builtin_amdgcn_ds_read_tr16_b64_v4i16((AS3 s16x4*)p); }
DI bf16x8 vread(lds_cptr p) {
  s16x4 a = vtr(p), b = vtr(p + 128);
  return __builtin_bit_cast(bf16x8, __builtin_shufflevector(a, b, 0, 1, 2, 3, 4, 5, 6, 7));
}
#define MFMA(a, b, c) __builtin_amdgcn_mfma_f32_32x32x16_bf16(a, b, c, 0, 0, 0)
DI float max3(float a, float b, float c) { return __builtin_fmaxf(__builtin_fmaxf(a, b), c); }
DI float max16(const f32x16& s) {
  const float a = max3(s[0], s[1], s[2]), b = max3(s[3], s[4], s[5]), c = max3(s[6], s[7], s[8]), d = max3(s[9], s[10], s[11]), e = max3(s[12], s[13], s[14]);
  return __builtin_fmaxf(max3(a, b, c), max3(d, e, s[15]));
}
DI float xhalf_max(float m) {
  auto rr = __builtin_amdgcn_permlane32_swap(__float_as_uint(m), __float_as_uint(m), false, false);
  return __builtin_fmaxf(__uint_as_float(rr[0]), __uint_as_float(rr[1]));
}

#define S_(x) #x
#define STR(x) S_(x)
#define EX(A, i) "v_exp_f32 v[" STR(A) "+" STR(i) "], v[" STR(A) "+" STR(i) "]\n\t"
#define SMF(VA, P) "v_smfmac_f32_32x32x32_bf16 v[0:15], v[" STR(VA) ":" STR(VA) "+3], v[" STR(P) ":" STR(P) "+7], v80\n\t"
#define QK(B) "v_mfma_f32_32x32x16_bf16 v[" STR(B) ":" STR(B) "+15], v[64:67], v[76:79], v[16:31]\n\t"
#define VRD(R, off) "ds_read_b64_tr_b16 v[" STR(R) ":" STR(R) "+1], v82 offset:" STR(off) "\n\tds_read_b64_tr_b16 v[" STR(R) "+2:" STR(R) "+3], v82 offset:" STR(off) "+128\n\t"
#define KRD(off) "ds_read_b128 v[64:67], v81 offset:" STR(off) "\n\t"
#define TILE_HEAD(A, B) EX(A, 0) EX(A, 1) "s_waitcnt lgkmcnt(0)\n\t" SMF(72, B + 8)
#define TILE_REST(A, B, i) VRD(72, (i) * 1024 + 512) EX(A, 2) EX(A, 3) EX(A, 4) EX(A, 5) EX(A, 6) QK(B) KRD(((i) + 2) * 1024) \
  EX(A, 7) EX(A, 8) EX(A, 9) EX(A, 10) EX(A, 11) SMF(68, A) VRD(68, ((i) + 1) * 1024) EX(A, 12) EX(A, 13) EX(A, 14) EX(A, 15)
#define TILE_E(i) TILE_HEAD(32, 48) TILE_REST(32, 48, i)
#define TILE_O(i) TILE_HEAD(48, 32) TILE_REST(48, 32, i)
#define MAIN_LOOP \
  "s_mov_b32 %[j], 0\n\t" \
  "s_setprio 3\n\t" \
  "s_nop 9\n\t" \
  EX(32, 0) EX(32, 1) \
  "s_branch 2f\n\t" \
  "1:\n\t" \
  TILE_HEAD(32, 48) \
  "2:\n\t" \
  TILE_REST(32, 48, 0) TILE_O(1) TILE_E(2) TILE_O(3) TILE_E(4) TILE_O(5) \
  "s_cmp_gt_i32 %[rdy], %[j]\n\t" \
  "s_cbranch_scc1 4f\n\t" \
  "3:\n\t" \
  "ds_read_b32 v85, v84\n\t" \
  "s_waitcnt lgkmcnt(0)\n\t" \
  "v_readfirstlane_b32 %[rdy], v85\n\t" \
  "s_nop 3\n\t" \
  "s_cmp_gt_i32 %[rdy], %[j]\n\t" \
  "s_cbranch_scc0 3b\n\t" \
  "4:\n\t" \
  "s_cmp_lg_u32 %[j], 7\n\t" \
  "s_cbranch_scc1 5f\n\t" \
  "s_setprio 0\n\t" \
  "5:\n\t" \
  TILE_E(6) TILE_O(7) \
  "v_add_u32 v81, 0x2000, v81\n\t" \
  "v_add_u32 v82, v83, v82\n\t" \
  "s_add_u32 %[j], %[j], 1\n\t" \
  "s_cmp_lt_u32 %[j], 5\n\t" \
  "s_cbranch_scc1 1b\n\t" \
  "s_setprio 2\n\t" \
  "s_cmp_lt_u32 %[j], 7\n\t" \
  "s_cbranch_scc1 1b\n\t" \
  "s_setprio 1\n\t" \
  "s_cmp_lt_u32 %[j], 8\n\t" \
  "s_cbranch_scc1 1b\n\t" \
  "s_setprio 3\n\t" \
  "s_waitcnt lgkmcnt(0)\n\t" \
  SMF(72, 48 + 8) \
  "s_nop 11\n\t"

__global__ __launch_bounds__(576, 2) void attn_fwd(const __hip_bfloat16* __restrict__ q, const __hip_bfloat16* __restrict__ k,
                                                   const __hip_bfloat16* __restrict__ v, __hip_bfloat16* __restrict__ out
                                                   ) {
  __shared__ __attribute__((aligned(1024))) char lds[LDS_BYTES];
  unsigned long long st[8];
  STAMP(0);
  const int tid = threadIdx.x, lane = tid & 63, r32 = lane & 31, hi = lane >> 5;
  const int wid = __builtin_amdgcn_readfirstlane(tid >> 6);
  const int b = blockIdx.x, head = (b & 7) * 4 + (b >> 6), qb = (b >> 3) & 7;
  const unsigned lds0 = (unsigned)(uintptr_t)lds;
  const unsigned hoff = head * (SEQ * DH * 2);
  unsigned koff = hoff + wid * 1024 + r32 * 32 + hi * 16, voff = hoff + wid * 1024 + lane * 16;
  unsigned qoff = koff + qb * 8192, aoff = hoff + hi * 16;
  unsigned mk = lds0 + LDS_K + wid * 1024, mv = lds0 + LDS_V + wid * 1024;
  asm volatile("" : "+v"(koff), "+v"(voff), "+v"(qoff), "+v"(aoff), "+s"(mk), "+s"(mv));
  SBAR();
  asm volatile("" : "+s"(q), "+s"(k), "+s"(v), "+s"(out));
  volatile AS3 int* flag = (volatile AS3 int*)((lds_ptr)lds + LDS_FLAG);
  if (wid == 8) {
    *flag = 0;
    const char* kb = (const char*)k; const char* vb = (const char*)v;
    __builtin_amdgcn_s_sleep(2);
#pragma unroll
    for (int i = 0; i < 8; ++i) { glds16(kb + i * 1024, koff, mk + i * 1024); glds16(vb + i * 1024, voff, mv + i * 1024); }
    asm volatile("s_waitcnt lgkmcnt(0)\n\ts_barrier" ::: "memory");
    for (int c = 1; c < 7; ++c) {
#pragma unroll
      for (int i = 0; i < 8; ++i) { glds16(kb + (c * 8 + i) * 1024, koff, mk + (c * 8 + i) * 1024); glds16(vb + (c * 8 + i) * 1024, voff, mv + (c * 8 + i) * 1024); }
      asm volatile("s_waitcnt vmcnt(16)" ::: "memory");
      *flag = c;
    }
    asm volatile("s_waitcnt vmcnt(0)" ::: "memory");
    *flag = 8;
    return;
  }
  u32x4 qraw, araw;
  asm volatile("global_load_dwordx4 %0, %2, %4\n\tglobal_load_dwordx4 %1, %3, %5" : "=&v"(qraw), "=&v"(araw) : "v"(qoff), "v"(aoff), "s"(q), "s"(k) : "memory");
  glds16(k, koff, mk);
  glds16(v, voff, mv);
  STAMP(1);
  { const unsigned one = (tid & 1) ? 0u : 0x3f80u;
    u32x4 ones = {one, 0u, one, 0u};
    *(AS3 u32x4*)((lds_ptr)lds + LDS_ONES + tid * 16) = ones;
    if (tid < 64) *(AS3 u32x4*)((lds_ptr)lds + LDS_ONES + 8192 + tid * 16) = ones; }
  unsigned ka0 = lds0 + LDS_K + lane * 16;
  const bool ones_lane = (lane >> 4) & 1;
  unsigned va0 = lds0 + (ones_lane ? LDS_ONES + 128 : LDS_V + hi * 256) + (lane & 15) * 8;
  int vstep = ones_lane ? 0 : 8192;
  int idx = 0xDDDD;
  unsigned fa = lds0 + LDS_FLAG;
  f32x16 o = {};
  asm volatile("" : "+{v[0:15]}"(o), "+{v80}"(idx), "+{v83}"(vstep), "+{v84}"(fa), "+v"(ka0), "+v"(va0));
  const f32x16 zero = {};
  asm volatile("s_waitcnt vmcnt(2)" : "+v"(qraw), "+v"(araw) :: "memory");
  STAMP(2);
  bf16x8 qf;
  { u32x4 w;
#pragma unroll
    for (int i = 0; i < 4; ++i) w[i] = cvtpk(__uint_as_float(qraw[i] << 16) * QSCALE, __uint_as_float(qraw[i] & 0xffff0000u) * QSCALE);
    qf = __builtin_bit_cast(bf16x8, w); }
  const lds_cptr kp = (lds_cptr)(uintptr_t)ka0, vp = (lds_cptr)(uintptr_t)va0;
  f32x16 negm = MFMA(__builtin_bit_cast(bf16x8, araw ^ 0x80008000u), qf, zero);
  asm volatile("s_waitcnt vmcnt(0) lgkmcnt(0)\n\ts_barrier" ::: "memory");
  STAMP(3);
  int ready = 0;
  for (int attempt = 0;; ++attempt) {
    bf16x8 k0 = kread(kp), kf = kread(kp + 1024), va = vread(vp);
    f32x16 sa = MFMA(k0, qf, negm);
    unsigned ka = ka0, vad = va0; int j;
    STAMP(4);
    asm volatile(MAIN_LOOP
      : "+{v[0:15]}"(o), "+{v[32:47]}"(sa), "+{v[64:67]}"(kf), "+{v[68:71]}"(va), "+{v81}"(ka), "+{v82}"(vad), [rdy] "+s"(ready), [j] "=&s"(j)
      : "{v[16:31]}"(negm), "{v[76:79]}"(qf), "{v80}"(idx), "{v83}"(vstep), "{v84}"(fa)
      : "v48", "v49", "v50", "v51", "v52", "v53", "v54", "v55", "v56", "v57", "v58", "v59", "v60", "v61", "v62", "v63", "v72", "v73", "v74", "v75", "v85", "scc", "memory");
    STAMP(5);
    const float rl = __builtin_amdgcn_rcpf(o[8]);
    unsigned w0 = cvtpk(o[0] * rl, o[1] * rl), w1 = cvtpk(o[2] * rl, o[3] * rl), w2 = cvtpk(o[4] * rl, o[5] * rl), w3 = cvtpk(o[6] * rl, o[7] * rl);
    auto r0 = __builtin_amdgcn_permlane32_swap(w0, w2, false, false);
    auto r1 = __builtin_amdgcn_permlane32_swap(w1, w3, false, false);
    u32x4 res = {r0[0], r1[0], r0[1], r1[1]};
    asm volatile("global_store_dwordx4 %0, %1, %2 sc1\n\ts_nop 1" :: "v"(qoff), "v"(res), "s"(out) : "memory");
    SBAR();
    float chk = o[8];
#pragma unroll
    for (int i = 0; i < 8; ++i) chk = __builtin_fmaf(__builtin_fabsf(o[i]), 0x1p-40f, chk);
    if (attempt || !__any(!(chk < 0x1p60f))) break;
    float mm = -INFINITY;
    for (int t = 0; t < NT; ++t) mm = __builtin_fmaxf(mm, max16(MFMA(kread(kp + t * 1024), qf, zero)));
    const float m = xhalf_max(mm);
#pragma unroll
    for (int i = 0; i < 16; ++i) negm[i] = -m;
    o = zero;
  }
  { unsigned long long tend = __builtin_amdgcn_s_memrealtime() + 0 * (st[1] - st[0]); while (__builtin_amdgcn_s_memrealtime() < tend) __builtin_amdgcn_s_sleep(1); }
}

extern "C" void kernel_launch(void* const* d_in, const int* in_sizes, int n_in, void* d_out, int out_size, void* d_ws, size_t ws_size, hipStream_t stream) {
  attn_fwd<<<256, 576, 0, stream>>>((const __hip_bfloat16*)d_in[0], (const __hip_bfloat16*)d_in[1], (const __hip_bfloat16*)d_in[2], (__hip_bfloat16*)d_out
  );
}
```

```cpp
#include <hip/hip_runtime.h>
#include <hip/hip_bf16.h>
#include <stdint.h>

typedef __bf16 bf16x8 __attribute__((ext_vector_type(8)));
typedef __bf16 bf16x16 __attribute__((ext_vector_type(16)));
typedef __bf16 bf16x2 __attribute__((ext_vector_type(2)));
typedef short s16x4 __attribute__((ext_vector_type(4)));
typedef float f32x2 __attribute__((ext_vector_type(2)));
typedef float f32x8 __attribute__((ext_vector_type(8)));
typedef float f32x16 __attribute__((ext_vector_type(16)));
typedef unsigned u32x2 __attribute__((ext_vector_type(2)));
typedef unsigned u32x4 __attribute__((ext_vector_type(4)));
#define AS3 __attribute__((address_space(3)))
typedef AS3 const char* lds_cptr;

constexpr int NT = 64;
constexpr unsigned LDS_K = 0, LDS_V = 65536, LDS_ONES = 131072, LDS_FLAG = LDS_ONES + 9216;
constexpr unsigned LDS_EX = LDS_FLAG + 256, EX_SLOT = 2304, LDS_BYTES = LDS_EX + 8 * EX_SLOT;
constexpr float QSCALE = 0.25f * 1.4426950408889634f;

#define DI __device__ __forceinline__
#define SBAR() __builtin_amdgcn_sched_barrier(0)
#define STAMP(i)

DI unsigned cvtpk(float lo, float hi) { f32x2 v = {lo, hi}; bf16x2 b = __builtin_convertvector(v, bf16x2); return __builtin_bit_cast(unsigned, b); }
DI bf16x8 kread(unsigned a) { return *(const AS3 bf16x8*)(uintptr_t)a; }
DI s16x4 vtr(unsigned a) { return __builtin_amdgcn_ds_read_tr16_b64_v4i16((AS3 s16x4*)(uintptr_t)a); }
DI bf16x8 vread(unsigned a) {
  s16x4 x = vtr(a), y = vtr(a + 128);
  return __builtin_bit_cast(bf16x8, __builtin_shufflevector(x, y, 0, 1, 2, 3, 4, 5, 6, 7));
}
#define MFMA(a, b, c) __builtin_amdgcn_mfma_f32_32x32x16_bf16(a, b, c, 0, 0, 0)
DI void store_rows(const f32x16& o, unsigned off, void* out) {
  const float rl = __builtin_amdgcn_rcpf(o[8]);
  unsigned w0 = cvtpk(o[0] * rl, o[1] * rl), w1 = cvtpk(o[2] * rl, o[3] * rl), w2 = cvtpk(o[4] * rl, o[5] * rl), w3 = cvtpk(o[6] * rl, o[7] * rl);
  auto r0 = __builtin_amdgcn_permlane32_swap(w0, w2, false, false);
  auto r1 = __builtin_amdgcn_permlane32_swap(w1, w3, false, false);
  u32x4 res = {r0[0], r1[0], r0[1], r1[1]};
  asm volatile("global_store_dwordx4 %0, %1, %2 sc1\n\ts_nop 1" :: "v"(off), "v"(res), "s"(out) : "memory");
}

#define S_(x) #x
#define STR(x) S_(x)
#define EX(A, i) "v_exp_f32 v[" STR(A) "+" STR(i) "], v[" STR(A) "+" STR(i) "]\n\t"
#define SMF(O, VA, P) "v_smfmac_f32_32x32x32_bf16 v[" STR(O) ":" STR(O) "+15], v[" STR(VA) ":" STR(VA) "+3], v[" STR(P) ":" STR(P) "+7], v116\n\t"
#define QK(B, Q, M) "v_mfma_f32_32x32x16_bf16 v[" STR(B) ":" STR(B) "+15], v[96:99], v[" STR(Q) ":" STR(Q) "+3], v[" STR(M) ":" STR(M) "+15]\n\t"
#define VRD(R, off) "ds_read_b64_tr_b16 v[" STR(R) ":" STR(R) "+1], v118 offset:" STR(off) "\n\tds_read_b64_tr_b16 v[" STR(R) "+2:" STR(R) "+3], v118 offset:" STR(off) "+128\n\t"
#define KRD(off) "ds_read_b128 v[96:99], v117 offset:" STR(off) "\n\t"
#define EX5(A, i) EX(A, (i)) EX(A, (i) + 1) EX(A, (i) + 2) EX(A, (i) + 3) EX(A, (i) + 4)
#define EX4(A, i) EX(A, (i)) EX(A, (i) + 1) EX(A, (i) + 2) EX(A, (i) + 3)
#define Y_HEAD EX(32, 0) EX(32, 1) SMF(0, 104, 48 + 8)
#define Y_REST(cur, next) VRD(104, (cur) + 512) EX5(32, 2) QK(48, 108, 64) KRD(next) EX5(32, 7) "s_waitcnt lgkmcnt(3)\n\t" SMF(16, 100, 32) EX4(32, 12)
#define X_STEP(next, EXTRA) EX(48, 0) EX(48, 1) "s_waitcnt lgkmcnt(0)\n\t" SMF(16, 104, 32 + 8) EX5(48, 2) QK(32, 112, 80) EX5(48, 7) SMF(0, 100, 48) VRD(100, next) EXTRA EX4(48, 12)
#define SEND_OY \
  "s_cmp_lg_u32 %[j], 7\n\t" \
  "s_cbranch_scc1 6f\n\t" \
  "ds_write_b128 %[exw], v[16:19]\n\t" \
  "ds_write_b128 %[exw], v[20:23] offset:1024\n\t" \
  "ds_write_b32 %[exs], v24\n\t" \
  "ds_write_b32 %[fw], v116\n\t" \
  "6:\n\t"
#define MAIN_LOOP \
  "s_waitcnt vmcnt(0) lgkmcnt(0)\n\t" \
  "s_barrier\n\t" \
  KRD(0) \
  "ds_read_b128 v[108:111], %[exr]\n\t" \
  "ds_read_b128 v[48:51], %[ax]\n\t" \
  VRD(100, 0) \
  "s_mov_b32 %[j], 0\n\t" \
  "s_mov_b32 %[rdy], 0\n\t" \
  "s_setprio 3\n\t" \
  "s_waitcnt lgkmcnt(4)\n\t" \
  QK(32, 112, 80) \
  "s_waitcnt lgkmcnt(2)\n\t" \
  "v_xor_b32 v52, %[sgn], v108\n\tv_xor_b32 v53, %[sgn], v109\n\tv_xor_b32 v54, %[sgn], v110\n\tv_xor_b32 v55, %[sgn], v111\n\t" \
  "s_nop 1\n\t" \
  "v_mfma_f32_32x32x16_bf16 v[64:79], v[48:51], v[52:55], 0\n\t" \
  "s_nop 3\n\t" \
  EX(32, 0) EX(32, 1) \
  "s_branch 2f\n\t" \
  "1:\n\t" \
  Y_HEAD \
  "2:\n\t" \
  Y_REST(0, 1024) X_STEP(1024, "") Y_HEAD Y_REST(1024, 2048) X_STEP(2048, "") Y_HEAD Y_REST(2048, 3072) X_STEP(3072, "") \
  "s_cmp_gt_i32 %[rdy], %[j]\n\t" \
  "s_cbranch_scc1 4f\n\t" \
  "3:\n\t" \
  "ds_read_b32 v121, v120\n\t" \
  "s_waitcnt lgkmcnt(0)\n\t" \
  "v_readfirstlane_b32 %[rdy], v121\n\t" \
  "s_nop 3\n\t" \
  "s_cmp_gt_i32 %[rdy], %[j]\n\t" \
  "s_cbranch_scc0 3b\n\t" \
  "4:\n\t" \
  "s_cmp_lg_u32 %[j], 7\n\t" \
  "s_cbranch_scc1 5f\n\t" \
  "s_setprio 0\n\t" \
  "5:\n\t" \
  Y_HEAD Y_REST(3072, 8192) X_STEP(8192, SEND_OY) \
  "v_add_u32 v117, 0x2000, v117\n\t" \
  "v_add_u32 v118, v119, v118\n\t" \
  "s_add_u32 %[j], %[j], 1\n\t" \
  "s_cmp_lt_u32 %[j], 5\n\t" \
  "s_cbranch_scc1 1b\n\t" \
  "s_setprio 2\n\t" \
  "s_cmp_lt_u32 %[j], 7\n\t" \
  "s_cbranch_scc1 1b\n\t" \
  "s_setprio 1\n\t" \
  "s_cmp_lt_u32 %[j], 8\n\t" \
  "s_cbranch_scc1 1b\n\t" \
  "s_setprio 3\n\t" \
  SMF(0, 104, 48 + 8) \
  "s_branch 8f\n\t" \
  "7:\n\t" \
  "s_sleep 1\n\t" \
  "8:\n\t" \
  "ds_read_b32 v121, %[fr]\n\t" \
  "ds_read_b128 v[16:19], %[exr]\n\t" \
  "ds_read_b128 v[20:23], %[exr] offset:1024\n\t" \
  "ds_read_b32 v24, %[ext]\n\t" \
  "s_waitcnt lgkmcnt(3)\n\t" \
  "v_cmp_eq_u32 vcc, v121, v116\n\t" \
  "s_nop 4\n\t" \
  "s_waitcnt lgkmcnt(0)\n\t" \
  "s_cbranch_vccz 7b\n\t"

#define L4 \
  "s_mov_b32 m0, %[t]\n\t" \
  "s_add_u32 s20, s20, 0x1000\n\t" \
  "s_addc_u32 s21, s21, 0\n\t" \
  "global_load_lds_dwordx4 %[ko], s[20:21]\n\t" \
  "global_load_lds_dwordx4 %[ko], s[20:21] offset:1024\n\t" \
  "global_load_lds_dwordx4 %[ko], s[20:21] offset:2048\n\t" \
  "global_load_lds_dwordx4 %[ko], s[20:21] offset:3072\n\t" \
  "s_bitset1_b32 m0, 16\n\t" \
  "s_add_u32 s22, s22, 0x1000\n\t" \
  "s_addc_u32 s23, s23, 0\n\t" \
  "global_load_lds_dwordx4 %[vo], s[22:23]\n\t" \
  "global_load_lds_dwordx4 %[vo], s[22:23] offset:1024\n\t" \
  "global_load_lds_dwordx4 %[vo], s[22:23] offset:2048\n\t" \
  "global_load_lds_dwordx4 %[vo], s[22:23] offset:3072\n\t" \
  "s_add_u32 %[t], %[t], 0x1000\n\t"
#define LOADER \
  "s_mov_b32 m0, 0x12000\n\t" \
  "s_setprio 3\n\t" \
  "global_load_lds_dwordx4 %[vo], s[22:23] offset:1024\n\t" \
  "global_load_lds_dwordx4 %[vo], s[22:23] offset:2048\n\t" \
  "global_load_lds_dwordx4 %[vo], s[22:23] offset:3072\n\t" \
  "s_bitset0_b32 m0, 16\n\t" \
  "s_mov_b32 %[t], 0x3000\n\t" \
  "global_load_lds_dwordx4 %[ko], s[20:21] offset:1024\n\t" \
  "global_load_lds_dwordx4 %[ko], s[20:21] offset:2048\n\t" \
  "global_load_lds_dwordx4 %[ko], s[20:21] offset:3072\n\t" \
  "v_mov_b32 %[x], 0\n\t" \
  "s_waitcnt lgkmcnt(0)\n\t" \
  "s_barrier\n\t" \
  "1:\n\t" \
  L4 \
  "s_cmp_eq_u32 %[t], 0x10000\n\t" \
  "s_cbranch_scc1 2f\n\t" \
  L4 \
  "v_add_u32 %[x], 1, %[x]\n\t" \
  "s_waitcnt vmcnt(8)\n\t" \
  "ds_write_b32 %[fa], %[x]\n\t" \
  "s_branch 1b\n\t" \
  "2:\n\t" \
  "v_mov_b32 %[x], 8\n\t" \
  "s_waitcnt vmcnt(0)\n\t" \
  "ds_write_b32 %[fa], %[x]\n\t"

__global__ __launch_bounds__(576, 2) void attn_fwd(const __hip_bfloat16* __restrict__ q, const __hip_bfloat16* __restrict__ k,
                                                   const __hip_bfloat16* __restrict__ v, __hip_bfloat16* __restrict__ out
                                                   ) {
  __shared__ __attribute__((aligned(1024))) char lds[LDS_BYTES];
  STAMP(0);
  const unsigned tid = threadIdx.x, b = blockIdx.x;
  const unsigned hoff = (b & 31) << 16, qboff = (b >> 5) << 13;
  const unsigned lin = tid << 4, wk = lin & 0x3c00;
  unsigned koff = hoff | wk | ((tid & 31) << 5) | ((tid >> 1) & 16);
  unsigned voff = hoff | lin, qoff = koff + qboff;
  unsigned mk = __builtin_amdgcn_readfirstlane(wk) + (unsigned)(uintptr_t)lds;
  asm volatile("" : "+v"(koff), "+v"(voff), "+v"(qoff), "+s"(mk));
  SBAR();
  u32x4 qraw;
  asm volatile("s_mov_b32 m0, %[mk]\n\ts_cmp_eq_u32 %[mk], 0x2000\n\tglobal_load_lds_dwordx4 %[ko], %[kp]\n\ts_bitset1_b32 m0, 16\n\ts_cbranch_scc1 1f\n\t"
               "global_load_dwordx4 v[56:59], %[qo], %[qp]\n\t1:\n\tglobal_load_lds_dwordx4 %[vo], %[vp]"
               : "=&{v[56:59]}"(qraw), "+s"(out) : [mk] "s"(mk), [ko] "v"(koff), [vo] "v"(voff), [qo] "v"(qoff), [qp] "s"(q), [kp] "s"(k), [vp] "s"(v) : "scc", "memory");
  STAMP(1);
  *(AS3 u32x2*)(uintptr_t)(LDS_ONES + lin) = u32x2{((tid & 1) - 1) & 0x3f80u, 0u};
  unsigned fa = LDS_FLAG;
  unsigned fw = LDS_FLAG + 64 + (wk >> 8);
  *(AS3 unsigned*)(uintptr_t)(mk == 0x2000 ? fa : fw) = 0u;
  if (mk == 0x2000) {
    unsigned t, x;
    asm volatile(LOADER : "+{s[20:21]}"(k), "+{s[22:23]}"(v), [t] "=&s"(t), [x] "=&v"(x) : [ko] "v"(koff), [vo] "v"(voff), [fa] "v"(fa) : "scc", "memory");
    return;
  }
  const unsigned lane = tid & 63, l16 = lane << 4, kh4 = wk & 0x1000;
  const unsigned om = (unsigned)-(int)((tid >> 4) & 1);
  const unsigned va0 = (om & (LDS_ONES + 128 + ((tid & 2) << 3))) | (~om & (LDS_V + ((tid & 32) << 3) + ((tid & 15) << 3)));
  unsigned ka = LDS_K + l16 + kh4, vad = va0 + (~om & kh4), vstep = ~om & 8192, idx = 0xDDDD;
  const unsigned exb = LDS_EX + (mk >> 10) * EX_SLOT, exp_ = LDS_EX + ((mk >> 10) ^ 4) * EX_SLOT;
  unsigned exw = exb + l16, exs = exb + 2048 + lane * 4, exr = exp_ + l16, ext = exp_ + 2048 + lane * 4, fr = fw ^ 16;
  unsigned ay = LDS_K + wk + ((tid & 32) << 4), ax = ay ^ 0x1000;
  f32x16 ox, oy;
  asm volatile("v_mov_b64 v[32:33], 0\n\ts_nop 1\n\tv_mfma_f32_32x32x8_bf16 v[0:15], v[32:33], v[32:33], 0\n\tv_mfma_f32_32x32x8_bf16 v[16:31], v[32:33], v[32:33], 0\n\t"
               "s_waitcnt vmcnt(2)"
               : "={v[0:15]}"(ox), "={v[16:31]}"(oy), "+{v116}"(idx), "+{v119}"(vstep), "+{v120}"(fa), "+{v117}"(ka), "+{v118}"(vad),
                 "+v"(exw), "+v"(exs), "+v"(exr), "+v"(ext), "+v"(fw), "+v"(fr), "+v"(ay), "+v"(ax) :: "v32", "v33", "memory");
  const bf16x8 kay = kread(ay);
  asm volatile("s_waitcnt vmcnt(1)" : "+{v[56:59]}"(qraw) :: "memory");
  STAMP(2);
  u32x4 qyw;
#pragma unroll
  for (int i = 0; i < 4; ++i) qyw[i] = cvtpk(__uint_as_float(qraw[i] << 16) * QSCALE, __uint_as_float(qraw[i] & 0xffff0000u) * QSCALE);
  *(AS3 u32x4*)(uintptr_t)exw = qyw;
  const f32x16 zero = {};
  f32x16 negy = MFMA(kay, __builtin_bit_cast(bf16x8, qyw ^ 0x80008000u), zero);
  bf16x8 qx;
  STAMP(3);
  STAMP(4);
  { int ready, j;
    asm volatile(MAIN_LOOP
      : "+{v[0:15]}"(ox), "+{v[16:31]}"(oy), "=&{v[108:111]}"(qx), "+{v117}"(ka), "+{v118}"(vad), [rdy] "=&s"(ready), [j] "=&s"(j)
      : "{v[80:95]}"(negy), "{v[112:115]}"(qyw), "{v116}"(idx), "{v119}"(vstep), "{v120}"(fa), [sgn] "s"(0x80008000u),
        [exw] "v"(exw), [exs] "v"(exs), [exr] "v"(exr), [ext] "v"(ext), [fw] "v"(fw), [fr] "v"(fr), [ax] "v"(ax)
      : "v32", "v33", "v34", "v35", "v36", "v37", "v38", "v39", "v40", "v41", "v42", "v43", "v44", "v45", "v46", "v47",
        "v48", "v49", "v50", "v51", "v52", "v53", "v54", "v55", "v56", "v57", "v58", "v59", "v60", "v61", "v62", "v63",
        "v64", "v65", "v66", "v67", "v68", "v69", "v70", "v71", "v72", "v73", "v74", "v75", "v76", "v77", "v78", "v79",
        "v96", "v97", "v98", "v99", "v100", "v101", "v102", "v103", "v104", "v105", "v106", "v107", "v121", "vcc", "scc", "memory"); }
  STAMP(5);
  const unsigned ooff = qoff ^ 0x1000;
  f32x16 o;
#pragma unroll
  for (int i = 0; i < 9; ++i) o[i] = ox[i] + oy[i];
  store_rows(o, ooff, out);
  SBAR();
  float chk = o[8];
#pragma unroll
  for (int i = 0; i < 8; ++i) chk = __builtin_fmaf(__builtin_fabsf(o[i]), 0x1p-40f, chk);
  if (__builtin_expect(__any(!(chk < 0x1p60f)), 0)) {
    float m = -INFINITY;
#pragma unroll 1
    for (int t = 0; t < NT; ++t) { const f32x16 s = MFMA(kread(LDS_K + l16 + t * 1024), qx, zero);
#pragma unroll
      for (int i = 0; i < 16; ++i) m = __builtin_fmaxf(m, s[i]); }
    { auto rr = __builtin_amdgcn_permlane32_swap(__float_as_uint(m), __float_as_uint(m), false, false);
      m = __builtin_fmaxf(__uint_as_float(rr[0]), __uint_as_float(rr[1])); }
    o = zero;
#pragma unroll 1
    for (int t = 0; t < NT; ++t) { const f32x16 s = MFMA(kread(LDS_K + l16 + t * 1024), qx, zero);
      const unsigned vt = va0 + (~om & (t * 1024));
#pragma unroll
      for (int h = 0; h < 2; ++h) { f32x8 p;
#pragma unroll
        for (int i = 0; i < 8; ++i) p[i] = __builtin_amdgcn_exp2f(s[8 * h + i] - m);
        o = __builtin_amdgcn_smfmac_f32_32x32x32_bf16(vread(vt + h * 512), __builtin_bit_cast(bf16x16, p), o, idx, 0, 0); } }
    store_rows(o, ooff, out);
  }
}

extern "C" void kernel_launch(void* const* d_in, const int* in_sizes, int n_in, void* d_out, int out_size, void* d_ws, size_t ws_size, hipStream_t stream) {
  attn_fwd<<<256, 576, 0, stream>>>((const __hip_bfloat16*)d_in[0], (const __hip_bfloat16*)d_in[1], (const __hip_bfloat16*)d_in[2], (__hip_bfloat16*)d_out
  );
}
```

```cpp
#include <hip/hip_runtime.h>
#include <hip/hip_bf16.h>
#include <stdint.h>

typedef __bf16 bf16x8 __attribute__((ext_vector_type(8)));
typedef __bf16 bf16x16 __attribute__((ext_vector_type(16)));
typedef __bf16 bf16x2 __attribute__((ext_vector_type(2)));
typedef short s16x4 __attribute__((ext_vector_type(4)));
typedef float f32x2 __attribute__((ext_vector_type(2)));
typedef float f32x8 __attribute__((ext_vector_type(8)));
typedef float f32x16 __attribute__((ext_vector_type(16)));
typedef unsigned u32x2 __attribute__((ext_vector_type(2)));
typedef unsigned u32x4 __attribute__((ext_vector_type(4)));
#define AS3 __attribute__((address_space(3)))
typedef AS3 const char* lds_cptr;

constexpr int NT = 64;
constexpr unsigned LDS_K = 0, LDS_V = 65536, LDS_ONES = 131072, LDS_FLAG = LDS_ONES + 9216;
constexpr unsigned LDS_EX = LDS_FLAG + 256, EX_SLOT = 2304, LDS_BYTES = LDS_EX + 8 * EX_SLOT;
constexpr float QSCALE = 0.25f * 1.4426950408889634f;

#define DI __device__ __forceinline__
#define SBAR() __builtin_amdgcn_sched_barrier(0)
#define STAMP(i)

DI unsigned cvtpk(float lo, float hi) { f32x2 v = {lo, hi}; bf16x2 b = __builtin_convertvector(v, bf16x2); return __builtin_bit_cast(unsigned, b); }
DI bf16x8 kread(unsigned a) { return *(const AS3 bf16x8*)(uintptr_t)a; }
DI s16x4 vtr(unsigned a) { return __builtin_amdgcn_ds_read_tr16_b64_v4i16((AS3 s16x4*)(uintptr_t)a); }
DI bf16x8 vread(unsigned a) {
  s16x4 x = vtr(a), y = vtr(a + 128);
  return __builtin_bit_cast(bf16x8, __builtin_shufflevector(x, y, 0, 1, 2, 3, 4, 5, 6, 7));
}
#define MFMA(a, b, c) __builtin_amdgcn_mfma_f32_32x32x16_bf16(a, b, c, 0, 0, 0)
DI void store_rows(const f32x16& o, unsigned off, void* out) {
  const float rl = __builtin_amdgcn_rcpf(o[8]);
  unsigned w0 = cvtpk(o[0] * rl, o[1] * rl), w1 = cvtpk(o[2] * rl, o[3] * rl), w2 = cvtpk(o[4] * rl, o[5] * rl), w3 = cvtpk(o[6] * rl, o[7] * rl);
  auto r0 = __builtin_amdgcn_permlane32_swap(w0, w2, false, false);
  auto r1 = __builtin_amdgcn_permlane32_swap(w1, w3, false, false);
  u32x4 res = {r0[0], r1[0], r0[1], r1[1]};
  asm volatile("global_store_dwordx4 %0, %1, %2 sc1\n\ts_nop 1" :: "v"(off), "v"(res), "s"(out) : "memory");
}

#define S_(x) #x
#define STR(x) S_(x)
#define EX(A, i) "v_exp_f32 v[" STR(A) "+" STR(i) "], v[" STR(A) "+" STR(i) "]\n\t"
#define SMF(O, VA, P) "v_smfmac_f32_32x32x32_bf16 v[" STR(O) ":" STR(O) "+15], v[" STR(VA) ":" STR(VA) "+3], v[" STR(P) ":" STR(P) "+7], v116\n\t"
#define QK(B, Q, M) "v_mfma_f32_32x32x16_bf16 v[" STR(B) ":" STR(B) "+15], v[96:99], v[" STR(Q) ":" STR(Q) "+3], v[" STR(M) ":" STR(M) "+15]\n\t"
#define VRD(R, off) "ds_read_b64_tr_b16 v[" STR(R) ":" STR(R) "+1], v118 offset:" STR(off) "\n\tds_read_b64_tr_b16 v[" STR(R) "+2:" STR(R) "+3], v118 offset:" STR(off) "+128\n\t"
#define KRD(off) "ds_read_b128 v[96:99], v117 offset:" STR(off) "\n\t"
#define EX5(A, i) EX(A, (i)) EX(A, (i) + 1) EX(A, (i) + 2) EX(A, (i) + 3) EX(A, (i) + 4)
#define EX4(A, i) EX(A, (i)) EX(A, (i) + 1) EX(A, (i) + 2) EX(A, (i) + 3)
#define Y_HEAD EX(32, 0) EX(32, 1) SMF(0, 104, 48 + 8)
#define Y_REST(cur, next) VRD(104, (cur) + 512) EX5(32, 2) QK(48, 108, 64) KRD(next) EX5(32, 7) "s_waitcnt lgkmcnt(3)\n\t" SMF(16, 100, 32) EX4(32, 12)
#define X_STEP(next, EXTRA) EX(48, 0) EX(48, 1) "s_waitcnt lgkmcnt(0)\n\t" SMF(16, 104, 32 + 8) EX5(48, 2) QK(32, 112, 80) EX5(48, 7) SMF(0, 100, 48) VRD(100, next) EXTRA EX4(48, 12)
#define SEND_OY \
  "s_cmp_lg_u32 %[j], 7\n\t" \
  "s_cbranch_scc1 6f\n\t" \
  "ds_write_b128 %[exw], v[16:19]\n\t" \
  "ds_write_b128 %[exw], v[20:23] offset:1024\n\t" \
  "ds_write_b32 %[exs], v24\n\t" \
  "ds_write_b32 %[fw], v116\n\t" \
  "6:\n\t"
#define MAIN_LOOP \
  "s_waitcnt vmcnt(0) lgkmcnt(0)\n\t" \
  "s_barrier\n\t" \
  KRD(0) \
  "ds_read_b128 v[108:111], %[exw]\n\t" \
  "ds_read_b128 v[48:51], %[ax]\n\t" \
  VRD(100, 0) \
  "s_mov_b32 %[j], 0\n\t" \
  "s_mov_b32 %[rdy], 0\n\t" \
  "s_setprio 3\n\t" \
  "s_waitcnt lgkmcnt(4)\n\t" \
  QK(32, 112, 80) \
  "s_waitcnt lgkmcnt(2)\n\t" \
  "v_xor_b32 v52, %[sgn], v108\n\tv_xor_b32 v53, %[sgn], v109\n\tv_xor_b32 v54, %[sgn], v110\n\tv_xor_b32 v55, %[sgn], v111\n\t" \
  "s_nop 1\n\t" \
  "v_mfma_f32_32x32x16_bf16 v[64:79], v[48:51], v[52:55], 0\n\t" \
  "s_nop 3\n\t" \
  EX(32, 0) EX(32, 1) \
  "s_branch 2f\n\t" \
  "1:\n\t" \
  Y_HEAD \
  "2:\n\t" \
  Y_REST(0, 1024) X_STEP(1024, "") Y_HEAD Y_REST(1024, 2048) X_STEP(2048, "") Y_HEAD Y_REST(2048, 3072) X_STEP(3072, "") \
  "s_cmp_gt_i32 %[rdy], %[j]\n\t" \
  "s_cbranch_scc1 4f\n\t" \
  "3:\n\t" \
  "ds_read_b32 v121, v120\n\t" \
  "s_waitcnt lgkmcnt(0)\n\t" \
  "v_readfirstlane_b32 %[rdy], v121\n\t" \
  "s_nop 3\n\t" \
  "s_cmp_gt_i32 %[rdy], %[j]\n\t" \
  "s_cbranch_scc0 3b\n\t" \
  "4:\n\t" \
  "s_cmp_lg_u32 %[j], 7\n\t" \
  "s_cbranch_scc1 5f\n\t" \
  "s_setprio 0\n\t" \
  "5:\n\t" \
  Y_HEAD Y_REST(3072, 8192) X_STEP(8192, SEND_OY) \
  "v_add_u32 v117, 0x2000, v117\n\t" \
  "v_add_u32 v118, v119, v118\n\t" \
  "s_add_u32 %[j], %[j], 1\n\t" \
  "s_cmp_lt_u32 %[j], 5\n\t" \
  "s_cbranch_scc1 1b\n\t" \
  "s_setprio 2\n\t" \
  "s_cmp_lt_u32 %[j], 7\n\t" \
  "s_cbranch_scc1 1b\n\t" \
  "s_setprio 1\n\t" \
  "s_cmp_lt_u32 %[j], 8\n\t" \
  "s_cbranch_scc1 1b\n\t" \
  "s_setprio 3\n\t" \
  SMF(0, 104, 48 + 8) \
  "s_branch 8f\n\t" \
  "7:\n\t" \
  "s_sleep 1\n\t" \
  "8:\n\t" \
  "ds_read_b32 v121, %[fr]\n\t" \
  "ds_read_b128 v[16:19], %[exr]\n\t" \
  "ds_read_b128 v[20:23], %[exr] offset:1024\n\t" \
  "ds_read_b32 v24, %[ext]\n\t" \
  "s_waitcnt lgkmcnt(3)\n\t" \
  "v_cmp_eq_u32 vcc, v121, v116\n\t" \
  "s_nop 4\n\t" \
  "s_waitcnt lgkmcnt(0)\n\t" \
  "s_cbranch_vccz 7b\n\t"

#define L4 \
  "s_mov_b32 m0, %[t]\n\t" \
  "s_add_u32 s20, s20, 0x1000\n\t" \
  "s_addc_u32 s21, s21, 0\n\t" \
  "global_load_lds_dwordx4 %[ko], s[20:21]\n\t" \
  "global_load_lds_dwordx4 %[ko], s[20:21] offset:1024\n\t" \
  "global_load_lds_dwordx4 %[ko], s[20:21] offset:2048\n\t" \
  "global_load_lds_dwordx4 %[ko], s[20:21] offset:3072\n\t" \
  "s_bitset1_b32 m0, 16\n\t" \
  "s_add_u32 s22, s22, 0x1000\n\t" \
  "s_addc_u32 s23, s23, 0\n\t" \
  "global_load_lds_dwordx4 %[vo], s[22:23]\n\t" \
  "global_load_lds_dwordx4 %[vo], s[22:23] offset:1024\n\t" \
  "global_load_lds_dwordx4 %[vo], s[22:23] offset:2048\n\t" \
  "global_load_lds_dwordx4 %[vo], s[22:23] offset:3072\n\t" \
  "s_add_u32 %[t], %[t], 0x1000\n\t"
#define LMODE 0
#define LSTART "s_waitcnt lgkmcnt(0)\n\ts_barrier\n\t" L4
#define LOADER \
  "s_setprio 3\n\t" \
  "s_mov_b32 %[t], 0x2000\n\t" \
  "s_sub_u32 s20, s20, 0x1000\n\t" \
  "s_subb_u32 s21, s21, 0\n\t" \
  "s_sub_u32 s22, s22, 0x1000\n\t" \
  "s_subb_u32 s23, s23, 0\n\t" \
  "v_mov_b32 %[x], 0\n\t" \
  LSTART \
  "1:\n\t" \
  L4 \
  "s_cmp_eq_u32 %[t], 0x10000\n\t" \
  "s_cbranch_scc1 2f\n\t" \
  L4 \
  "v_add_u32 %[x], 1, %[x]\n\t" \
  "s_waitcnt vmcnt(8)\n\t" \
  "ds_write_b32 %[fa], %[x]\n\t" \
  "s_branch 1b\n\t" \
  "2:\n\t" \
  "v_mov_b32 %[x], 8\n\t" \
  "s_waitcnt vmcnt(0)\n\t" \
  "ds_write_b32 %[fa], %[x]\n\t"

__global__ __launch_bounds__(576, 2) void attn_fwd(const __hip_bfloat16* __restrict__ q, const __hip_bfloat16* __restrict__ k,
                                                   const __hip_bfloat16* __restrict__ v, __hip_bfloat16* __restrict__ out
                                                   ) {
  __shared__ __attribute__((aligned(1024))) char lds[LDS_BYTES];
  STAMP(0);
  const unsigned tid = threadIdx.x, b = blockIdx.x;
  const unsigned hoff = (b & 31) << 16, qboff = (b >> 5) << 13;
  const unsigned lin = tid << 4, wk = lin & 0x3c00;
  unsigned koff = hoff | wk | ((tid & 31) << 5) | ((tid >> 1) & 16);
  unsigned voff = hoff | lin, qoff = koff + qboff;
  unsigned mk = __builtin_amdgcn_readfirstlane(wk) + (unsigned)(uintptr_t)lds;
  asm volatile("" : "+v"(koff), "+v"(voff), "+v"(qoff), "+s"(mk));
  SBAR();
  u32x4 qraw;
  asm volatile("s_mov_b32 m0, %[mk]\n\ts_cmp_eq_u32 %[mk], 0x2000\n\ts_cbranch_scc1 1f\n\tglobal_load_lds_dwordx4 %[ko], %[kp]\n\ts_bitset1_b32 m0, 16\n\t"
               "global_load_dwordx4 v[56:59], %[qo], %[qp]\n\tglobal_load_lds_dwordx4 %[vo], %[vp]\n\t1:"
               : "=&{v[56:59]}"(qraw), "+s"(out) : [mk] "s"(mk), [ko] "v"(koff), [vo] "v"(voff), [qo] "v"(qoff), [qp] "s"(q), [kp] "s"(k), [vp] "s"(v) : "scc", "memory");
  STAMP(1);
  *(AS3 u32x2*)(uintptr_t)(LDS_ONES + lin) = u32x2{((tid & 1) - 1) & 0x3f80u, 0u};
  unsigned fa = LDS_FLAG;
  unsigned fw = LDS_FLAG + 64 + (wk >> 8);
  *(AS3 unsigned*)(uintptr_t)(mk == 0x2000 ? fa : fw) = 0u;
  if (mk == 0x2000) {
    unsigned t, x;
    asm volatile(LOADER : "+{s[20:21]}"(k), "+{s[22:23]}"(v), [t] "=&s"(t), [x] "=&v"(x) : [ko] "v"(koff), [vo] "v"(voff), [fa] "v"(fa) : "scc", "memory");
    return;
  }
  const unsigned lane = tid & 63, l16 = lane << 4, kh4 = wk & 0x1000;
  const unsigned om = (unsigned)-(int)((tid >> 4) & 1);
  const unsigned va0 = (om & (LDS_ONES + 128 + ((tid & 2) << 3))) | (~om & (LDS_V + ((tid & 32) << 3) + ((tid & 15) << 3)));
  unsigned ka = LDS_K + l16 + kh4, vad = va0 + (~om & kh4), vstep = ~om & 8192, idx = 0xDDDD;
  const unsigned exb = LDS_EX + (mk >> 10) * EX_SLOT, exp_ = LDS_EX + ((mk >> 10) ^ 4) * EX_SLOT;
  unsigned exw = exb + l16, exs = exb + 2048 + lane * 4, exr = exp_ + l16, ext = exp_ + 2048 + lane * 4, fr = fw ^ 16;
  unsigned ay = LDS_K + wk + ((tid & 32) << 4), ax = ay ^ 0x1000;
  f32x16 ox, oy;
  asm volatile("v_mov_b64 v[32:33], 0\n\ts_nop 1\n\tv_mfma_f32_32x32x8_bf16 v[0:15], v[32:33], v[32:33], 0\n\tv_mfma_f32_32x32x8_bf16 v[16:31], v[32:33], v[32:33], 0\n\t"
               "s_waitcnt vmcnt(2)"
               : "={v[0:15]}"(ox), "={v[16:31]}"(oy), "+{v116}"(idx), "+{v119}"(vstep), "+{v120}"(fa), "+{v117}"(ka), "+{v118}"(vad),
                 "+v"(exw), "+v"(exs), "+v"(exr), "+v"(ext), "+v"(fw), "+v"(fr), "+v"(ay), "+v"(ax) :: "v32", "v33", "memory");
  const bf16x8 kay = kread(ay);
  asm volatile("s_waitcnt vmcnt(1)" : "+{v[56:59]}"(qraw) :: "memory");
  STAMP(2);
  u32x4 qyw;
#pragma unroll
  for (int i = 0; i < 4; ++i) qyw[i] = cvtpk(__uint_as_float(qraw[i] << 16) * QSCALE, __uint_as_float(qraw[i] & 0xffff0000u) * QSCALE);
  *(AS3 u32x4*)(uintptr_t)exr = qyw;
  const f32x16 zero = {};
  f32x16 negy = MFMA(kay, __builtin_bit_cast(bf16x8, qyw ^ 0x80008000u), zero);
  bf16x8 qx;
  STAMP(3);
  STAMP(4);
  { int ready, j;
    asm volatile(MAIN_LOOP
      : "+{v[0:15]}"(ox), "+{v[16:31]}"(oy), "=&{v[108:111]}"(qx), "+{v117}"(ka), "+{v118}"(vad), [rdy] "=&s"(ready), [j] "=&s"(j)
      : "{v[80:95]}"(negy), "{v[112:115]}"(qyw), "{v116}"(idx), "{v119}"(vstep), "{v120}"(fa), [sgn] "s"(0x80008000u),
        [exw] "v"(exw), [exs] "v"(exs), [exr] "v"(exr), [ext] "v"(ext), [fw] "v"(fw), [fr] "v"(fr), [ax] "v"(ax)
      : "v32", "v33", "v34", "v35", "v36", "v37", "v38", "v39", "v40", "v41", "v42", "v43", "v44", "v45", "v46", "v47",
        "v48", "v49", "v50", "v51", "v52", "v53", "v54", "v55", "v56", "v57", "v58", "v59", "v60", "v61", "v62", "v63",
        "v64", "v65", "v66", "v67", "v68", "v69", "v70", "v71", "v72", "v73", "v74", "v75", "v76", "v77", "v78", "v79",
        "v96", "v97", "v98", "v99", "v100", "v101", "v102", "v103", "v104", "v105", "v106", "v107", "v121", "vcc", "scc", "memory"); }
  STAMP(5);
  const unsigned ooff = qoff ^ 0x1000;
  f32x16 o;
#pragma unroll
  for (int i = 0; i < 9; ++i) o[i] = ox[i] + oy[i];
  store_rows(o, ooff, out);
  SBAR();
  float chk = o[8];
#pragma unroll
  for (int i = 0; i < 8; ++i) chk = __builtin_fmaf(__builtin_fabsf(o[i]), 0x1p-40f, chk);
  if (__builtin_expect(__any(!(chk < 0x1p60f)), 0)) {
    float m = -INFINITY;
#pragma unroll 1
    for (int t = 0; t < NT; ++t) { const f32x16 s = MFMA(kread(LDS_K + l16 + t * 1024), qx, zero);
#pragma unroll
      for (int i = 0; i < 16; ++i) m = __builtin_fmaxf(m, s[i]); }
    { auto rr = __builtin_amdgcn_permlane32_swap(__float_as_uint(m), __float_as_uint(m), false, false);
      m = __builtin_fmaxf(__uint_as_float(rr[0]), __uint_as_float(rr[1])); }
    o = zero;
#pragma unroll 1
    for (int t = 0; t < NT; ++t) { const f32x16 s = MFMA(kread(LDS_K + l16 + t * 1024), qx, zero);
      const unsigned vt = va0 + (~om & (t * 1024));
#pragma unroll
      for (int h = 0; h < 2; ++h) { f32x8 p;
#pragma unroll
        for (int i = 0; i < 8; ++i) p[i] = __builtin_amdgcn_exp2f(s[8 * h + i] - m);
        o = __builtin_amdgcn_smfmac_f32_32x32x32_bf16(vread(vt + h * 512), __builtin_bit_cast(bf16x16, p), o, idx, 0, 0); } }
    store_rows(o, ooff, out);
  }
}

extern "C" void kernel_launch(void* const* d_in, const int* in_sizes, int n_in, void* d_out, int out_size, void* d_ws, size_t ws_size, hipStream_t stream) {
  attn_fwd<<<256, 576, 0, stream>>>((const __hip_bfloat16*)d_in[0], (const __hip_bfloat16*)d_in[1], (const __hip_bfloat16*)d_in[2], (__hip_bfloat16*)d_out
  );
}
```

```cpp
#include <hip/hip_runtime.h>
#include <hip/hip_bf16.h>
#include <stdint.h>

typedef __bf16 bf16x8 __attribute__((ext_vector_type(8)));
typedef __bf16 bf16x16 __attribute__((ext_vector_type(16)));
typedef __bf16 bf16x2 __attribute__((ext_vector_type(2)));
typedef short s16x4 __attribute__((ext_vector_type(4)));
typedef float f32x2 __attribute__((ext_vector_type(2)));
typedef float f32x8 __attribute__((ext_vector_type(8)));
typedef float f32x16 __attribute__((ext_vector_type(16)));
typedef unsigned u32x2 __attribute__((ext_vector_type(2)));
typedef unsigned u32x4 __attribute__((ext_vector_type(4)));
#define AS3 __attribute__((address_space(3)))

constexpr int NT = 64;
constexpr unsigned LDS_K = 0, LDS_V = 65536, LDS_ONES = 131072, LDS_FLAG = LDS_ONES + 9216;
constexpr unsigned LDS_EX = LDS_FLAG + 256, EX_SLOT = 2304, LDS_BYTES = LDS_EX + 8 * EX_SLOT;
constexpr float QSCALE = 0.25f * 1.4426950408889634f;

#define DI __device__ __forceinline__
#define SBAR() __builtin_amdgcn_sched_barrier(0)
#define MFMA(a, b, c) __builtin_amdgcn_mfma_f32_32x32x16_bf16(a, b, c, 0, 0, 0)

DI unsigned cvtpk(float lo, float hi) { f32x2 v = {lo, hi}; bf16x2 b = __builtin_convertvector(v, bf16x2); return __builtin_bit_cast(unsigned, b); }
DI bf16x8 kread(unsigned a) { return *(const AS3 bf16x8*)(uintptr_t)a; }
DI s16x4 vtr(unsigned a) { return __builtin_amdgcn_ds_read_tr16_b64_v4i16((AS3 s16x4*)(uintptr_t)a); }
DI bf16x8 vread(unsigned a) {
  s16x4 x = vtr(a), y = vtr(a + 128);
  return __builtin_bit_cast(bf16x8, __builtin_shufflevector(x, y, 0, 1, 2, 3, 4, 5, 6, 7));
}
DI void store_rows(const f32x16& o, unsigned off, void* out) {
  const float rl = __builtin_amdgcn_rcpf(o[8]);
  unsigned w0 = cvtpk(o[0] * rl, o[1] * rl), w1 = cvtpk(o[2] * rl, o[3] * rl), w2 = cvtpk(o[4] * rl, o[5] * rl), w3 = cvtpk(o[6] * rl, o[7] * rl);
  auto r0 = __builtin_amdgcn_permlane32_swap(w0, w2, false, false);
  auto r1 = __builtin_amdgcn_permlane32_swap(w1, w3, false, false);
  u32x4 res = {r0[0], r1[0], r0[1], r1[1]};
  asm volatile("global_store_dwordx4 %0, %1, %2 sc1\n\ts_nop 1" :: "v"(off), "v"(res), "s"(out) : "memory");
}

#define S_(x) #x
#define STR(x) S_(x)
#define EX(A, i) "v_exp_f32 v[" STR(A) "+" STR(i) "], v[" STR(A) "+" STR(i) "]\n\t"
#define EX4(A, i) EX(A, (i)) EX(A, (i) + 1) EX(A, (i) + 2) EX(A, (i) + 3)
#define EX5(A, i) EX4(A, i) EX(A, (i) + 4)
#define SMF(O, VA, P) "v_smfmac_f32_32x32x32_bf16 v[" STR(O) ":" STR(O) "+15], v[" STR(VA) ":" STR(VA) "+3], v[" STR(P) ":" STR(P) "+7], v116\n\t"
#define QK(B, Q, M) "v_mfma_f32_32x32x16_bf16 v[" STR(B) ":" STR(B) "+15], v[96:99], v[" STR(Q) ":" STR(Q) "+3], v[" STR(M) ":" STR(M) "+15]\n\t"
#define VRD(R, off) "ds_read_b64_tr_b16 v[" STR(R) ":" STR(R) "+1], v118 offset:" STR(off) "\n\tds_read_b64_tr_b16 v[" STR(R) "+2:" STR(R) "+3], v118 offset:" STR(off) "+128\n\t"
#define KRD(off) "ds_read_b128 v[96:99], v117 offset:" STR(off) "\n\t"
#define Y_HEAD EX(32, 0) EX(32, 1) SMF(0, 104, 48 + 8)
#define Y_REST(cur, next) VRD(104, (cur) + 512) EX5(32, 2) QK(48, 108, 64) KRD(next) EX5(32, 7) "s_waitcnt lgkmcnt(3)\n\t" SMF(16, 100, 32) EX4(32, 12)
#define X_STEP(next, EXTRA) EX(48, 0) EX(48, 1) "s_waitcnt lgkmcnt(0)\n\t" SMF(16, 104, 32 + 8) EX5(48, 2) QK(32, 112, 80) EX5(48, 7) SMF(0, 100, 48) VRD(100, next) EXTRA EX4(48, 12)
#define SEND_OY \
  "s_cmp_lg_u32 %[j], 7\n\t" \
  "s_cbranch_scc1 6f\n\t" \
  "ds_write_b128 %[exw], v[16:19]\n\t" \
  "ds_write_b128 %[exw], v[20:23] offset:1024\n\t" \
  "ds_write_b32 %[exs], v24\n\t" \
  "ds_write_b32 %[fw], v116\n\t" \
  "6:\n\t"
#define MAIN_LOOP \
  "s_waitcnt vmcnt(0) lgkmcnt(0)\n\t" \
  "s_barrier\n\t"                                                     \
  KRD(0) \
  "ds_read_b128 v[108:111], %[exw]\n\t" \
  "ds_read_b128 v[48:51], %[ax]\n\t" \
  VRD(100, 0) \
  "s_mov_b32 %[j], 0\n\t" \
  "s_mov_b32 %[rdy], 0\n\t" \
  "s_setprio 3\n\t" \
  "s_waitcnt lgkmcnt(4)\n\t" \
  QK(32, 112, 80) \
  "s_waitcnt lgkmcnt(2)\n\t" \
  "v_xor_b32 v52, %[sgn], v108\n\tv_xor_b32 v53, %[sgn], v109\n\tv_xor_b32 v54, %[sgn], v110\n\tv_xor_b32 v55, %[sgn], v111\n\t" \
  "s_nop 1\n\t" \
  "v_mfma_f32_32x32x16_bf16 v[64:79], v[48:51], v[52:55], 4.0\n\t"    \
  "s_nop 3\n\t" \
  EX(32, 0) EX(32, 1) \
  "s_branch 2f\n\t" \
  "1:\n\t" \
  Y_HEAD \
  "2:\n\t" \
  Y_REST(0, 1024) X_STEP(1024, "") Y_HEAD Y_REST(1024, 2048) X_STEP(2048, "") Y_HEAD Y_REST(2048, 3072) X_STEP(3072, "") \
  "s_cmp_gt_i32 %[rdy], %[j]\n\t"                                     \
  "s_cbranch_scc1 4f\n\t" \
  "3:\n\t" \
  "ds_read_b32 v121, v120\n\t" \
  "s_waitcnt lgkmcnt(0)\n\t" \
  "v_readfirstlane_b32 %[rdy], v121\n\t" \
  "s_nop 3\n\t" \
  "s_cmp_gt_i32 %[rdy], %[j]\n\t" \
  "s_cbranch_scc0 3b\n\t" \
  "4:\n\t" \
  "s_cmp_lg_u32 %[j], 7\n\t" \
  "s_cbranch_scc1 5f\n\t" \
  "s_setprio 0\n\t" \
  "5:\n\t" \
  Y_HEAD Y_REST(3072, 8192) X_STEP(8192, SEND_OY) \
  "v_add_u32 v117, 0x2000, v117\n\t" \
  "v_add_u32 v118, v119, v118\n\t" \
  "s_add_u32 %[j], %[j], 1\n\t" \
  "s_cmp_lt_u32 %[j], 5\n\t" \
  "s_cbranch_scc1 1b\n\t" \
  "s_setprio 2\n\t" \
  "s_cmp_lt_u32 %[j], 7\n\t" \
  "s_cbranch_scc1 1b\n\t" \
  "s_setprio 1\n\t" \
  "s_cmp_lt_u32 %[j], 8\n\t" \
  "s_cbranch_scc1 1b\n\t" \
  "s_setprio 3\n\t" \
  SMF(0, 104, 48 + 8) \
  "s_branch 8f\n\t" \
  "7:\n\t" \
  "s_sleep 1\n\t" \
  "8:\n\t"                                                            \
  "ds_read_b32 v121, %[fr]\n\t" \
  "ds_read_b128 v[16:19], %[exr]\n\t" \
  "ds_read_b128 v[20:23], %[exr] offset:1024\n\t" \
  "ds_read_b32 v24, %[ext]\n\t" \
  "s_waitcnt lgkmcnt(3)\n\t" \
  "v_cmp_eq_u32 vcc, v121, v116\n\t" \
  "s_nop 4\n\t" \
  "s_waitcnt lgkmcnt(0)\n\t" \
  "s_cbranch_vccz 7b\n\t"

#define L4 \
  "s_mov_b32 m0, %[t]\n\t" \
  "s_add_u32 s20, s20, 0x1000\n\t" \
  "s_addc_u32 s21, s21, 0\n\t" \
  "global_load_lds_dwordx4 %[ko], s[20:21]\n\t" \
  "global_load_lds_dwordx4 %[ko], s[20:21] offset:1024\n\t" \
  "global_load_lds_dwordx4 %[ko], s[20:21] offset:2048\n\t" \
  "global_load_lds_dwordx4 %[ko], s[20:21] offset:3072\n\t" \
  "s_bitset1_b32 m0, 16\n\t" \
  "s_add_u32 s22, s22, 0x1000\n\t" \
  "s_addc_u32 s23, s23, 0\n\t" \
  "global_load_lds_dwordx4 %[vo], s[22:23]\n\t" \
  "global_load_lds_dwordx4 %[vo], s[22:23] offset:1024\n\t" \
  "global_load_lds_dwordx4 %[vo], s[22:23] offset:2048\n\t" \
  "global_load_lds_dwordx4 %[vo], s[22:23] offset:3072\n\t" \
  "s_add_u32 %[t], %[t], 0x1000\n\t"
#define LOADER \
  "s_setprio 3\n\t" \
  "s_mov_b32 %[t], 0x2000\n\t" \
  "s_sub_u32 s20, s20, 0x1000\n\t" \
  "s_subb_u32 s21, s21, 0\n\t" \
  "s_sub_u32 s22, s22, 0x1000\n\t" \
  "s_subb_u32 s23, s23, 0\n\t" \
  "v_mov_b32 %[x], 0\n\t" \
  "s_waitcnt lgkmcnt(0)\n\t" \
  "s_barrier\n\t" \
  L4 \
  "1:\n\t" \
  L4 \
  "s_cmp_eq_u32 %[t], 0x10000\n\t" \
  "s_cbranch_scc1 2f\n\t" \
  L4 \
  "v_add_u32 %[x], 1, %[x]\n\t" \
  "s_waitcnt vmcnt(8)\n\t" \
  "ds_write_b32 %[fa], %[x]\n\t" \
  "s_branch 1b\n\t" \
  "2:\n\t" \
  "v_mov_b32 %[x], 8\n\t" \
  "s_waitcnt vmcnt(0)\n\t" \
  "ds_write_b32 %[fa], %[x]\n\t"

__global__ __launch_bounds__(576, 2) void attn_fwd(const __hip_bfloat16* __restrict__ q, const __hip_bfloat16* __restrict__ k,
                                                   const __hip_bfloat16* __restrict__ v, __hip_bfloat16* __restrict__ out) {
  __shared__ __attribute__((aligned(1024))) char lds[LDS_BYTES];
  const unsigned tid = threadIdx.x, b = blockIdx.x;
  const unsigned hoff = (b & 31) << 16, qboff = (b >> 5) << 13;
  const unsigned lin = tid << 4, wk = lin & 0x3c00;
  unsigned koff = hoff | wk | ((tid & 31) << 5) | ((tid >> 1) & 16);
  unsigned voff = hoff | lin, qoff = koff + qboff;
  unsigned mk = __builtin_amdgcn_readfirstlane(wk) + (unsigned)(uintptr_t)lds;
  asm volatile("" : "+v"(koff), "+v"(voff), "+v"(qoff), "+s"(mk));
  SBAR();
  u32x4 qraw;
  asm volatile("s_mov_b32 m0, %[mk]\n\ts_cmp_eq_u32 %[mk], 0x2000\n\ts_cbranch_scc1 1f\n\tglobal_load_lds_dwordx4 %[ko], %[kp]\n\ts_bitset1_b32 m0, 16\n\t"
               "global_load_dwordx4 v[56:59], %[qo], %[qp]\n\tglobal_load_lds_dwordx4 %[vo], %[vp]\n\t1:"
               : "=&{v[56:59]}"(qraw), "+s"(out) : [mk] "s"(mk), [ko] "v"(koff), [vo] "v"(voff), [qo] "v"(qoff), [qp] "s"(q), [kp] "s"(k), [vp] "s"(v) : "scc", "memory");
  *(AS3 u32x2*)(uintptr_t)(LDS_ONES + lin) = u32x2{((tid & 1) - 1) & 0x3f80u, 0u};
  unsigned fa = LDS_FLAG;
  unsigned fw = LDS_FLAG + 64 + (wk >> 8);
  *(AS3 unsigned*)(uintptr_t)(mk == 0x2000 ? fa : fw) = 0u;
  if (mk == 0x2000) {
    unsigned t, x;
    asm volatile(LOADER : "+{s[20:21]}"(k), "+{s[22:23]}"(v), [t] "=&s"(t), [x] "=&v"(x) : [ko] "v"(koff), [vo] "v"(voff), [fa] "v"(fa) : "scc", "memory");
    return;
  }
  const unsigned lane = tid & 63, l16 = lane << 4, kh4 = wk & 0x1000;
  const unsigned om = (unsigned)-(int)((tid >> 4) & 1);
  const unsigned va0 = (om & (LDS_ONES + 128 + ((tid & 2) << 3))) | (~om & (LDS_V + ((tid & 32) << 3) + ((tid & 15) << 3)));
  unsigned ka = LDS_K + l16 + kh4, vad = va0 + (~om & kh4), vstep = ~om & 8192, idx = 0xDDDD;
  const unsigned exb = LDS_EX + (mk >> 10) * EX_SLOT, exp_ = LDS_EX + ((mk >> 10) ^ 4) * EX_SLOT;
  unsigned exw = exb + l16, exs = exb + 2048 + lane * 4, exr = exp_ + l16, ext = exp_ + 2048 + lane * 4, fr = fw ^ 16;
  unsigned ay = LDS_K + wk + ((tid & 32) << 4), ax = ay ^ 0x1000;
  f32x16 ox, oy;
  asm volatile("v_mov_b64 v[32:33], 0\n\ts_nop 1\n\tv_mfma_f32_32x32x8_bf16 v[0:15], v[32:33], v[32:33], 0\n\tv_mfma_f32_32x32x8_bf16 v[16:31], v[32:33], v[32:33], 0\n\t"
               "s_waitcnt vmcnt(2)"
               : "={v[0:15]}"(ox), "={v[16:31]}"(oy), "+{v116}"(idx), "+{v119}"(vstep), "+{v120}"(fa), "+{v117}"(ka), "+{v118}"(vad),
                 "+v"(exw), "+v"(exs), "+v"(exr), "+v"(ext), "+v"(fw), "+v"(fr), "+v"(ay), "+v"(ax) :: "v32", "v33", "memory");
  const bf16x8 kay = kread(ay);
  asm volatile("s_waitcnt vmcnt(1)" : "+{v[56:59]}"(qraw) :: "memory");
  u32x4 qyw;
#pragma unroll
  for (int i = 0; i < 4; ++i) qyw[i] = cvtpk(__uint_as_float(qraw[i] << 16) * QSCALE, __uint_as_float(qraw[i] & 0xffff0000u) * QSCALE);
  *(AS3 u32x4*)(uintptr_t)exr = qyw;
  const f32x16 zero = {};
  f32x16 negy = MFMA(kay, __builtin_bit_cast(bf16x8, qyw ^ 0x80008000u), zero + 4.0f);
  bf16x8 qx;
  { int ready, j;
    asm volatile(MAIN_LOOP
      : "+{v[0:15]}"(ox), "+{v[16:31]}"(oy), "=&{v[108:111]}"(qx), "+{v117}"(ka), "+{v118}"(vad), [rdy] "=&s"(ready), [j] "=&s"(j)
      : "{v[80:95]}"(negy), "{v[112:115]}"(qyw), "{v116}"(idx), "{v119}"(vstep), "{v120}"(fa), [sgn] "s"(0x80008000u),
        [exw] "v"(exw), [exs] "v"(exs), [exr] "v"(exr), [ext] "v"(ext), [fw] "v"(fw), [fr] "v"(fr), [ax] "v"(ax)
      : "v32", "v33", "v34", "v35", "v36", "v37", "v38", "v39", "v40", "v41", "v42", "v43", "v44", "v45", "v46", "v47",
        "v48", "v49", "v50", "v51", "v52", "v53", "v54", "v55", "v56", "v57", "v58", "v59", "v60", "v61", "v62", "v63",
        "v64", "v65", "v66", "v67", "v68", "v69", "v70", "v71", "v72", "v73", "v74", "v75", "v76", "v77", "v78", "v79",
        "v96", "v97", "v98", "v99", "v100", "v101", "v102", "v103", "v104", "v105", "v106", "v107", "v121", "vcc", "scc", "memory"); }
  const unsigned ooff = qoff ^ 0x1000;
  f32x16 o;
#pragma unroll
  for (int i = 0; i < 9; ++i) o[i] = ox[i] + oy[i];
  store_rows(o, ooff, out);
  SBAR();
  float chk = o[8];
#pragma unroll
  for (int i = 0; i < 8; ++i) chk = __builtin_fmaf(__builtin_fabsf(o[i]), 0x1p-40f, chk);
  if (__builtin_expect(__any(!(chk < 0x1p60f)), 0)) {
    float m = -INFINITY;
#pragma unroll 1
    for (int t = 0; t < NT; ++t) { const f32x16 s = MFMA(kread(LDS_K + l16 + t * 1024), qx, zero);
#pragma unroll
      for (int i = 0; i < 16; ++i) m = __builtin_fmaxf(m, s[i]); }
    { auto rr = __builtin_amdgcn_permlane32_swap(__float_as_uint(m), __float_as_uint(m), false, false);
      m = __builtin_fmaxf(__uint_as_float(rr[0]), __uint_as_float(rr[1])); }
    o = zero;
#pragma unroll 1
    for (int t = 0; t < NT; ++t) { const f32x16 s = MFMA(kread(LDS_K + l16 + t * 1024), qx, zero);
      const unsigned vt = va0 + (~om & (t * 1024));
#pragma unroll
      for (int h = 0; h < 2; ++h) { f32x8 p;
#pragma unroll
        for (int i = 0; i < 8; ++i) p[i] = __builtin_amdgcn_exp2f(s[8 * h + i] - m);
        o = __builtin_amdgcn_smfmac_f32_32x32x32_bf16(vread(vt + h * 512), __builtin_bit_cast(bf16x16, p), o, idx, 0, 0); } }
    store_rows(o, ooff, out);
  }
}

extern "C" void kernel_launch(void* const* d_in, const int* in_sizes, int n_in, void* d_out, int out_size, void* d_ws, size_t ws_size, hipStream_t stream) {
  attn_fwd<<<256, 576, 0, stream>>>((const __hip_bfloat16*)d_in[0], (const __hip_bfloat16*)d_in[1], (const __hip_bfloat16*)d_in[2], (__hip_bfloat16*)d_out);
}
```

```cpp
#include <hip/hip_runtime.h>
#include <hip/hip_bf16.h>
#include <stdint.h>

typedef __bf16 bf16x8 __attribute__((ext_vector_type(8)));
typedef __bf16 bf16x16 __attribute__((ext_vector_type(16)));
typedef __bf16 bf16x2 __attribute__((ext_vector_type(2)));
typedef short s16x4 __attribute__((ext_vector_type(4)));
typedef float f32x2 __attribute__((ext_vector_type(2)));
typedef float f32x8 __attribute__((ext_vector_type(8)));
typedef float f32x16 __attribute__((ext_vector_type(16)));
typedef unsigned u32x2 __attribute__((ext_vector_type(2)));
typedef unsigned u32x4 __attribute__((ext_vector_type(4)));
#define AS3 __attribute__((address_space(3)))

constexpr int NT = 64;
constexpr unsigned LDS_K = 0, LDS_V = 65536, LDS_ONES = 131072, LDS_FLAG = LDS_ONES + 9216;
constexpr unsigned LDS_EX = LDS_FLAG + 256, EX_SLOT = 2304, LDS_BYTES = LDS_EX + 8 * EX_SLOT;
constexpr float QSCALE = 0.25f * 1.4426950408889634f;

#define DI __device__ __forceinline__
#define SBAR() __builtin_amdgcn_sched_barrier(0)
#define MFMA(a, b, c) __builtin_amdgcn_mfma_f32_32x32x16_bf16(a, b, c, 0, 0, 0)

DI unsigned cvtpk(float lo, float hi) { f32x2 v = {lo, hi}; bf16x2 b = __builtin_convertvector(v, bf16x2); return __builtin_bit_cast(unsigned, b); }
DI bf16x8 kread(unsigned a) { return *(const AS3 bf16x8*)(uintptr_t)a; }
DI s16x4 vtr(unsigned a) { return __builtin_amdgcn_ds_read_tr16_b64_v4i16((AS3 s16x4*)(uintptr_t)a); }
DI bf16x8 vread(unsigned a) {
  s16x4 x = vtr(a), y = vtr(a + 128);
  return __builtin_bit_cast(bf16x8, __builtin_shufflevector(x, y, 0, 1, 2, 3, 4, 5, 6, 7));
}
DI void store_rows(const f32x16& o, unsigned off, void* out) {
  const float rl = __builtin_amdgcn_rcpf(o[8]);
  unsigned w0 = cvtpk(o[0] * rl, o[1] * rl), w1 = cvtpk(o[2] * rl, o[3] * rl), w2 = cvtpk(o[4] * rl, o[5] * rl), w3 = cvtpk(o[6] * rl, o[7] * rl);
  auto r0 = __builtin_amdgcn_permlane32_swap(w0, w2, false, false);
  auto r1 = __builtin_amdgcn_permlane32_swap(w1, w3, false, false);
  u32x4 res = {r0[0], r1[0], r0[1], r1[1]};
  asm volatile("global_store_dwordx4 %0, %1, %2 sc1\n\ts_nop 1" :: "v"(off), "v"(res), "s"(out) : "memory");
}

#define S_(x) #x
#define STR(x) S_(x)
#define EX(A, i) "v_exp_f32 v[" STR(A) "+" STR(i) "], v[" STR(A) "+" STR(i) "]\n\t"
#define EX4(A, i) EX(A, (i)) EX(A, (i) + 1) EX(A, (i) + 2) EX(A, (i) + 3)
#define EX5(A, i) EX4(A, i) EX(A, (i) + 4)
#define SMF(O, VA, P) "v_smfmac_f32_32x32x32_bf16 v[" STR(O) ":" STR(O) "+15], v[" STR(VA) ":" STR(VA) "+3], v[" STR(P) ":" STR(P) "+7], v116\n\t"
#define QK(B, Q, M) "v_mfma_f32_32x32x16_bf16 v[" STR(B) ":" STR(B) "+15], v[96:99], v[" STR(Q) ":" STR(Q) "+3], v[" STR(M) ":" STR(M) "+15]\n\t"
#define VRD(R, off) "ds_read_b64_tr_b16 v[" STR(R) ":" STR(R) "+1], v118 offset:" STR(off) "\n\tds_read_b64_tr_b16 v[" STR(R) "+2:" STR(R) "+3], v118 offset:" STR(off) "+128\n\t"
#define KRD(off) "ds_read_b128 v[96:99], v117 offset:" STR(off) "\n\t"
#define Y_HEAD EX(32, 0) EX(32, 1) SMF(0, 104, 48 + 8)
#define Y_REST(cur, next) VRD(104, (cur) + 512) EX5(32, 2) QK(48, 108, 64) KRD(next) EX5(32, 7) "s_waitcnt lgkmcnt(3)\n\t" SMF(16, 100, 32) EX4(32, 12)
#define X_STEP(next, EXTRA) EX(48, 0) EX(48, 1) "s_waitcnt lgkmcnt(0)\n\t" SMF(16, 104, 32 + 8) EX5(48, 2) QK(32, 112, 80) EX5(48, 7) SMF(0, 100, 48) VRD(100, next) EXTRA EX4(48, 12)
#define SEND_OY \
  "s_cmp_lg_u32 %[j], 7\n\t" \
  "s_cbranch_scc1 6f\n\t" \
  "ds_write_b128 %[exw], v[16:19]\n\t" \
  "ds_write_b128 %[exw], v[20:23] offset:1024\n\t" \
  "ds_write_b32 %[exs], v24\n\t" \
  "ds_write_b32 %[fw], v116\n\t" \
  "6:\n\t"
#define MAIN_LOOP \
  "s_waitcnt vmcnt(0) lgkmcnt(0)\n\t" \
  "s_barrier\n\t"                                                     \
  KRD(0) \
  "ds_read_b128 v[108:111], %[exw]\n\t" \
  "ds_read_b128 v[48:51], %[ax]\n\t" \
  VRD(100, 0) \
  "s_mov_b32 %[j], 0\n\t" \
  "s_mov_b32 %[rdy], 0\n\t" \
  "s_setprio 3\n\t" \
  "s_waitcnt lgkmcnt(4)\n\t" \
  QK(32, 112, 80) \
  "s_waitcnt lgkmcnt(2)\n\t" \
  "v_xor_b32 v52, %[sgn], v108\n\tv_xor_b32 v53, %[sgn], v109\n\tv_xor_b32 v54, %[sgn], v110\n\tv_xor_b32 v55, %[sgn], v111\n\t" \
  "s_nop 1\n\t" \
  "v_mfma_f32_32x32x16_bf16 v[64:79], v[48:51], v[52:55], v[64:79]\n\t"    \
  "s_nop 3\n\t" \
  EX(32, 0) EX(32, 1) \
  "s_branch 2f\n\t" \
  "1:\n\t" \
  Y_HEAD \
  "2:\n\t" \
  Y_REST(0, 1024) X_STEP(1024, "") Y_HEAD Y_REST(1024, 2048) X_STEP(2048, "") Y_HEAD Y_REST(2048, 3072) X_STEP(3072, "") \
  "s_cmp_gt_i32 %[rdy], %[j]\n\t"                                     \
  "s_cbranch_scc1 4f\n\t" \
  "3:\n\t" \
  "ds_read_b32 v121, v120\n\t" \
  "s_waitcnt lgkmcnt(0)\n\t" \
  "v_readfirstlane_b32 %[rdy], v121\n\t" \
  "s_nop 3\n\t" \
  "s_cmp_gt_i32 %[rdy], %[j]\n\t" \
  "s_cbranch_scc0 3b\n\t" \
  "4:\n\t" \
  "s_cmp_lg_u32 %[j], 7\n\t" \
  "s_cbranch_scc1 5f\n\t" \
  "s_setprio 0\n\t" \
  "5:\n\t" \
  Y_HEAD Y_REST(3072, 8192) X_STEP(8192, SEND_OY) \
  "v_add_u32 v117, 0x2000, v117\n\t" \
  "v_add_u32 v118, v119, v118\n\t" \
  "s_add_u32 %[j], %[j], 1\n\t" \
  "s_cmp_lt_u32 %[j], 5\n\t" \
  "s_cbranch_scc1 1b\n\t" \
  "s_setprio 2\n\t" \
  "s_cmp_lt_u32 %[j], 7\n\t" \
  "s_cbranch_scc1 1b\n\t" \
  "s_setprio 1\n\t" \
  "s_cmp_lt_u32 %[j], 8\n\t" \
  "s_cbranch_scc1 1b\n\t" \
  "s_setprio 3\n\t" \
  SMF(0, 104, 48 + 8) \
  "s_branch 8f\n\t" \
  "7:\n\t" \
  "s_sleep 1\n\t" \
  "8:\n\t"                                                            \
  "ds_read_b32 v121, %[fr]\n\t" \
  "ds_read_b128 v[16:19], %[exr]\n\t" \
  "ds_read_b128 v[20:23], %[exr] offset:1024\n\t" \
  "ds_read_b32 v24, %[ext]\n\t" \
  "s_waitcnt lgkmcnt(3)\n\t" \
  "v_cmp_eq_u32 vcc, v121, v116\n\t" \
  "s_nop 4\n\t" \
  "s_waitcnt lgkmcnt(0)\n\t" \
  "s_cbranch_vccz 7b\n\t"

#define L4 \
  "s_mov_b32 m0, %[t]\n\t" \
  "s_add_u32 s20, s20, 0x1000\n\t" \
  "s_addc_u32 s21, s21, 0\n\t" \
  "global_load_lds_dwordx4 %[ko], s[20:21]\n\t" \
  "global_load_lds_dwordx4 %[ko], s[20:21] offset:1024\n\t" \
  "global_load_lds_dwordx4 %[ko], s[20:21] offset:2048\n\t" \
  "global_load_lds_dwordx4 %[ko], s[20:21] offset:3072\n\t" \
  "s_bitset1_b32 m0, 16\n\t" \
  "s_add_u32 s22, s22, 0x1000\n\t" \
  "s_addc_u32 s23, s23, 0\n\t" \
  "global_load_lds_dwordx4 %[vo], s[22:23]\n\t" \
  "global_load_lds_dwordx4 %[vo], s[22:23] offset:1024\n\t" \
  "global_load_lds_dwordx4 %[vo], s[22:23] offset:2048\n\t" \
  "global_load_lds_dwordx4 %[vo], s[22:23] offset:3072\n\t" \
  "s_add_u32 %[t], %[t], 0x1000\n\t"
#define LOADER \
  "s_setprio 3\n\t" \
  "s_mov_b32 %[t], 0x2000\n\t" \
  "s_sub_u32 s20, s20, 0x1000\n\t" \
  "s_subb_u32 s21, s21, 0\n\t" \
  "s_sub_u32 s22, s22, 0x1000\n\t" \
  "s_subb_u32 s23, s23, 0\n\t" \
  "v_mov_b32 %[x], 0\n\t" \
  "s_waitcnt lgkmcnt(0)\n\t" \
  "s_barrier\n\t" \
  L4 \
  "1:\n\t" \
  L4 \
  "s_cmp_eq_u32 %[t], 0x10000\n\t" \
  "s_cbranch_scc1 2f\n\t" \
  L4 \
  "v_add_u32 %[x], 1, %[x]\n\t" \
  "s_waitcnt vmcnt(8)\n\t" \
  "ds_write_b32 %[fa], %[x]\n\t" \
  "s_branch 1b\n\t" \
  "2:\n\t" \
  "v_mov_b32 %[x], 8\n\t" \
  "s_waitcnt vmcnt(0)\n\t" \
  "ds_write_b32 %[fa], %[x]\n\t"

__global__ __launch_bounds__(576, 2) void attn_fwd(const __hip_bfloat16* __restrict__ q, const __hip_bfloat16* __restrict__ k,
                                                   const __hip_bfloat16* __restrict__ v, __hip_bfloat16* __restrict__ out) {
  __shared__ __attribute__((aligned(1024))) char lds[LDS_BYTES];
  const unsigned tid = threadIdx.x, b = blockIdx.x;
  const unsigned hoff = (b & 31) << 16, qboff = (b >> 5) << 13;
  const unsigned lin = tid << 4, wk = lin & 0x3c00;
  unsigned koff = hoff | wk | ((tid & 31) << 5) | ((tid >> 1) & 16);
  unsigned voff = hoff | lin, qoff = koff + qboff;
  unsigned mk = __builtin_amdgcn_readfirstlane(wk) + (unsigned)(uintptr_t)lds;
  asm volatile("" : "+v"(koff), "+v"(voff), "+v"(qoff), "+s"(mk));
  SBAR();
  u32x4 qraw;
  asm volatile("s_mov_b32 m0, %[mk]\n\ts_cmp_eq_u32 %[mk], 0x2000\n\ts_cbranch_scc1 1f\n\tglobal_load_lds_dwordx4 %[ko], %[kp]\n\ts_bitset1_b32 m0, 16\n\t"
               "global_load_dwordx4 v[56:59], %[qo], %[qp]\n\tglobal_load_lds_dwordx4 %[vo], %[vp]\n\t1:"
               : "=&{v[56:59]}"(qraw), "+s"(out) : [mk] "s"(mk), [ko] "v"(koff), [vo] "v"(voff), [qo] "v"(qoff), [qp] "s"(q), [kp] "s"(k), [vp] "s"(v) : "scc", "memory");
  *(AS3 u32x2*)(uintptr_t)(LDS_ONES + lin) = u32x2{((tid & 1) - 1) & 0x3f80u, 0u};
  unsigned fa = LDS_FLAG;
  unsigned fw = LDS_FLAG + 64 + (wk >> 8);
  *(AS3 unsigned*)(uintptr_t)(mk == 0x2000 ? fa : fw) = 0u;
  if (mk == 0x2000) {
    unsigned t, x;
    asm volatile(LOADER : "+{s[20:21]}"(k), "+{s[22:23]}"(v), [t] "=&s"(t), [x] "=&v"(x) : [ko] "v"(koff), [vo] "v"(voff), [fa] "v"(fa) : "scc", "memory");
    return;
  }
  const unsigned lane = tid & 63, l16 = lane << 4, kh4 = wk & 0x1000;
  const unsigned om = (unsigned)-(int)((tid >> 4) & 1);
  const unsigned va0 = (om & (LDS_ONES + 128 + ((tid & 2) << 3))) | (~om & (LDS_V + ((tid & 32) << 3) + ((tid & 15) << 3)));
  unsigned ka = LDS_K + l16 + kh4, vad = va0 + (~om & kh4), vstep = ~om & 8192, idx = 0xDDDD;
  const unsigned exb = LDS_EX + (mk >> 10) * EX_SLOT, exp_ = LDS_EX + ((mk >> 10) ^ 4) * EX_SLOT;
  unsigned exw = exb + l16, exs = exb + 2048 + lane * 4, exr = exp_ + l16, ext = exp_ + 2048 + lane * 4, fr = fw ^ 16;
  unsigned ay = LDS_K + wk + ((tid & 32) << 4), ax = ay ^ 0x1000;
  f32x16 ox, oy, cx, cy;
  asm volatile("v_mov_b64 v[32:33], 0\n\tv_mov_b32 v34, 0x3f803f80\n\tv_mov_b32 v35, 0x3f803f80\n\t"
               "v_mfma_f32_32x32x8_bf16 v[0:15], v[32:33], v[32:33], 0\n\tv_mfma_f32_32x32x8_bf16 v[16:31], v[32:33], v[32:33], 0\n\t"
               "v_mfma_f32_32x32x8_bf16 v[80:95], v[34:35], v[34:35], 4.0\n\tv_mfma_f32_32x32x8_bf16 v[64:79], v[34:35], v[34:35], 4.0\n\ts_nop 7\n\t"
               "s_waitcnt vmcnt(2)"
               : "={v[0:15]}"(ox), "={v[16:31]}"(oy), "={v[64:79]}"(cx), "={v[80:95]}"(cy), "+{v116}"(idx), "+{v119}"(vstep), "+{v120}"(fa), "+{v117}"(ka), "+{v118}"(vad),
                 "+v"(exw), "+v"(exs), "+v"(exr), "+v"(ext), "+v"(fw), "+v"(fr), "+v"(ay), "+v"(ax) :: "v32", "v33", "v34", "v35", "memory");
  const bf16x8 kay = kread(ay);
  asm volatile("s_waitcnt vmcnt(1)" : "+{v[56:59]}"(qraw) :: "memory");
  u32x4 qyw;
#pragma unroll
  for (int i = 0; i < 4; ++i) qyw[i] = cvtpk(__uint_as_float(qraw[i] << 16) * QSCALE, __uint_as_float(qraw[i] & 0xffff0000u) * QSCALE);
  *(AS3 u32x4*)(uintptr_t)exr = qyw;
  const f32x16 zero = {};
  f32x16 negy = MFMA(kay, __builtin_bit_cast(bf16x8, qyw ^ 0x80008000u), cy);
  bf16x8 qx;
  { int ready, j;
    asm volatile(MAIN_LOOP
      : "+{v[0:15]}"(ox), "+{v[16:31]}"(oy), "=&{v[108:111]}"(qx), "+{v[64:79]}"(cx), "+{v117}"(ka), "+{v118}"(vad), [rdy] "=&s"(ready), [j] "=&s"(j)
      : "{v[80:95]}"(negy), "{v[112:115]}"(qyw), "{v116}"(idx), "{v119}"(vstep), "{v120}"(fa), [sgn] "s"(0x80008000u),
        [exw] "v"(exw), [exs] "v"(exs), [exr] "v"(exr), [ext] "v"(ext), [fw] "v"(fw), [fr] "v"(fr), [ax] "v"(ax)
      : "v32", "v33", "v34", "v35", "v36", "v37", "v38", "v39", "v40", "v41", "v42", "v43", "v44", "v45", "v46", "v47",
        "v48", "v49", "v50", "v51", "v52", "v53", "v54", "v55", "v56", "v57", "v58", "v59", "v60", "v61", "v62", "v63",
        "v96", "v97", "v98", "v99", "v100", "v101", "v102", "v103", "v104", "v105", "v106", "v107", "v121", "vcc", "scc", "memory"); }
  const unsigned ooff = qoff ^ 0x1000;
  f32x16 o;
#pragma unroll
  for (int i = 0; i < 9; ++i) o[i] = ox[i] + oy[i];
  store_rows(o, ooff, out);
  SBAR();
  float chk = o[8];
#pragma unroll
  for (int i = 0; i < 8; ++i) chk = __builtin_fmaf(__builtin_fabsf(o[i]), 0x1p-20f, chk);
  if (__builtin_expect(__any(!(chk < 0x1p100f) || !(o[8] >= 1.0f)), 0)) {
    float m = -INFINITY;
#pragma unroll 1
    for (int t = 0; t < NT; ++t) { const f32x16 s = MFMA(kread(LDS_K + l16 + t * 1024), qx, zero);
#pragma unroll
      for (int i = 0; i < 16; ++i) m = __builtin_fmaxf(m, s[i]); }
    { auto rr = __builtin_amdgcn_permlane32_swap(__float_as_uint(m), __float_as_uint(m), false, false);
      m = __builtin_fmaxf(__uint_as_float(rr[0]), __uint_as_float(rr[1])); }
    o = zero;
#pragma unroll 1
    for (int t = 0; t < NT; ++t) { const f32x16 s = MFMA(kread(LDS_K + l16 + t * 1024), qx, zero);
      const unsigned vt = va0 + (~om & (t * 1024));
#pragma unroll
      for (int h = 0; h < 2; ++h) { f32x8 p;
#pragma unroll
        for (int i = 0; i < 8; ++i) p[i] = __builtin_amdgcn_exp2f(s[8 * h + i] - m);
        o = __builtin_amdgcn_smfmac_f32_32x32x32_bf16(vread(vt + h * 512), __builtin_bit_cast(bf16x16, p), o, idx, 0, 0); } }
    store_rows(o, ooff, out);
  }
}

extern "C" void kernel_launch(void* const* d_in, const int* in_sizes, int n_in, void* d_out, int out_size, void* d_ws, size_t ws_size, hipStream_t stream) {
  attn_fwd<<<256, 576, 0, stream>>>((const __hip_bfloat16*)d_in[0], (const __hip_bfloat16*)d_in[1], (const __hip_bfloat16*)d_in[2], (__hip_bfloat16*)d_out);
}
```
